```python
import torch

def _softmax(x, axis=-1):
    return torch.softmax(x, dim=axis)
def _gelu(x):
    return torch.nn.functional.gelu(x, approximate="tanh")
def _silu(x):
    return torch.nn.functional.silu(x)
def _sigmoid(x):
    return torch.sigmoid(x)
def _layernorm(x, axis=-1, eps=1e-5):
    m = x.mean(dim=axis, keepdim=True)
    v = x.var(dim=axis, keepdim=True, unbiased=False)
    return (x - m) / torch.sqrt(v + eps)
def _scatter_add(tgt, idx, src, axis=0):
    return tgt.clone().index_add_(axis, idx, src)
def _segment_sum(src, seg, n):
    out = torch.zeros((n,) + src.shape[1:], dtype=src.dtype)
    return out.index_add_(0, seg, src)
def _pad(x, pw):
    flat = tuple(v for pair in reversed(pw) for v in pair)
    return torch.nn.functional.pad(x, flat)

kernel_name = 'concat_heads__grouped_kv_mqa__sliding_local_dilated__prompt'

def setup_inputs():

    _w = (lambda a: (lambda t: t.to(torch.bfloat16) if t.is_floating_point() else t)(torch.as_tensor(a)))
    def _A_setup():
        B, S, D, H = 1, 2048, 512, 32
        Dh = D // H
        return {
            "x": _w((lambda *s: torch.randn(*s, dtype=torch.bfloat16) * 0.05)(B, S, D)),
            "wq": _w((lambda *s: torch.randn(*s, dtype=torch.bfloat16) * 3.2)(D, H * Dh)),
            "wk": _w((lambda *s: torch.randn(*s, dtype=torch.bfloat16) * 3.2)(D, Dh)),
            "wv": _w((lambda *s: torch.randn(*s, dtype=torch.bfloat16) * 1.6)(D, Dh)),
            "wo": _w((lambda *s: torch.randn(*s, dtype=torch.bfloat16) * 1.6)(H * Dh, D)),
        }
    _A_in = _A_setup()

    def _B_setup():
        B, S, D = 1, 2048, 512
        return {
            "x": _w((lambda *s: torch.randn(*s, dtype=torch.bfloat16) * 0.1)(B, S, D)),
            "wqkv": _w((lambda *s: torch.randn(*s, dtype=torch.bfloat16) * 0.8)(D, 3 * D)),
            "wo": _w((lambda *s: torch.randn(*s, dtype=torch.bfloat16) * 0.8)(D, D)),
        }
    _B_in = _B_setup()

    return {
        "A_x": _A_in["x"],
        "A_wq": _A_in["wq"],
        "A_wk": _A_in["wk"],
        "A_wv": _A_in["wv"],
        "A_wo": _A_in["wo"],
        "B_x": _B_in["x"],
        "B_wqkv": _B_in["wqkv"],
        "B_wo": _B_in["wo"],
    }

def reference(A_x, A_wq, A_wk, A_wv, A_wo, B_x, B_wqkv, B_wo):
    def _A_ref(x, wq, wk, wv, wo):
        B, S, D = x.shape
        H = 32
        Dh = D // H
        q = (lambda x, *d: x.permute(*d))((x @ wq).reshape(B, S, H, Dh), 0, 2, 1, 3)
        k = x @ wk
        v = x @ wv
        scores = torch.einsum("bhqd,bkd->bhqk", q, k) * (Dh ** -0.5)
        p = _softmax(scores, axis=-1)
        o = torch.einsum("bhqk,bkd->bhqd", p, v)
        return (lambda x, *d: x.permute(*d))(o, 0, 2, 1, 3).reshape(B, S, H * Dh) @ wo

    def _B_ref(x, wqkv, wo):
        B, S, D = x.shape
        H = 32; Dh = D // H
        W, dil = 8, 2
        qkv = (lambda x, *d: x.permute(*d))((x @ wqkv).reshape(B, S, 3, H, Dh), 2, 0, 3, 1, 4)
        q, k, v = qkv[0], qkv[1], qkv[2]
        scale = Dh ** -0.5
        pos = torch.arange(S)
        off = torch.arange(W) * dil
        idx = pos[:, None] - off[None, :]
        mask = idx >= 0
        idx_c = torch.where(mask, idx, 0).reshape(-1)
        kg = k[:, :, idx_c, :].reshape(B, H, S, W, Dh)
        vg = v[:, :, idx_c, :].reshape(B, H, S, W, Dh)
        logits = torch.einsum("bhsd,bhswd->bhsw", q, kg) * scale
        logits = torch.where(mask[None, None, :, :], logits, -1e9)
        attn = _softmax(logits, axis=-1)
        o = torch.einsum("bhsw,bhswd->bhsd", attn, vg)
        return (lambda x, *d: x.permute(*d))(o, 0, 2, 1, 3).reshape(B, S, D) @ wo

    a = _A_ref(A_x, A_wq, A_wk, A_wv, A_wo)
    b = _B_ref(B_x, B_wqkv, B_wo)
    return torch.concatenate([a, b], axis=-1)
```

```cpp
#include <hip/hip_runtime.h>
#include <hip/hip_bf16.h>
#include <stdint.h>

typedef short bf16x8 __attribute__((ext_vector_type(8)));
typedef short s16x4 __attribute__((ext_vector_type(4)));
typedef float f32x16 __attribute__((ext_vector_type(16)));
typedef unsigned u32x4 __attribute__((ext_vector_type(4)));
typedef unsigned short u16;
#define LDSP __attribute__((address_space(3)))
#define GLBP __attribute__((address_space(1)))
#define DI __device__ __forceinline__
#define MFMA32(a, b, c) __builtin_amdgcn_mfma_f32_32x32x16_bf16((a), (b), (c), 0, 0, 0)

constexpr int S = 2048, DM = 512;
constexpr float QSCALE = 0.25f * 1.4426950408889634f;
constexpr int KIMG_BYTES = 64 * 1024, VGRP = 544, VIMG_BYTES = 128 * VGRP, KV_BYTES = KIMG_BYTES + VIMG_BYTES;
constexpr float MSHIFT = 64.f;

constexpr size_t WS_QA = 0, WS_KV = 2u << 20, WS_QKVB = 3u << 20, WS_OA = 10u << 20, WS_OB = 12u << 20;

typedef __bf16 bf16x2_t __attribute__((ext_vector_type(2)));
typedef float f32x2_t __attribute__((ext_vector_type(2)));
DI unsigned pk_bf16(float lo, float hi) { f32x2_t v = {lo, hi}; return __builtin_bit_cast(unsigned, __builtin_convertvector(v, bf16x2_t)); }
DI float bf_lo(unsigned u) { return __uint_as_float(u << 16); }
DI float bf_hi(unsigned u) { return __uint_as_float(u & 0xffff0000u); }
DI void glds16(const void* g, void* l) { __builtin_amdgcn_global_load_lds((const GLBP unsigned*)g, (LDSP unsigned*)l, 16, 0, 0); }
typedef short v4i16_t __attribute__((ext_vector_type(4)));
DI s16x4 tr_read(const char* p) { return __builtin_bit_cast(s16x4, __builtin_amdgcn_ds_read_tr16_b64_v4i16((LDSP v4i16_t*)p)); }
DI int crow(int r, int hi) { return (r & 3) + 8 * (r >> 2) + 4 * hi; }

template <int BN>
DI void gemm_tile(const u16* __restrict__ X, int row0, const u16* __restrict__ W, int ldw, int n0, char* lds, f32x16 (&acc)[BN / 64][2]) {
    constexpr int NC = BN / 64;
    constexpr int XB = 128 * 128;
    constexpr int WROW = BN * 2;
    constexpr int WB = 64 * WROW;
    constexpr int BUF = XB + WB;
    const int tid = threadIdx.x, l = tid & 63, w = __builtin_amdgcn_readfirstlane(tid >> 6);
    const int wr = w >> 1, wc = w & 1, h = l >> 5, r = l & 31;
    const u16* xsrc[4];
#pragma unroll
    for (int i = 0; i < 4; ++i) { const int row = 8 * (4 * w + i) + (l >> 3); const int c = (l & 7) ^ ((row >> 1) & 7); xsrc[i] = X + (size_t)(row0 + row) * DM + c * 8; }
    constexpr int WP = WB / 1024 / 4;
    constexpr int RPP = 1024 / WROW;
    constexpr int CPR = WROW / 16;
    const u16* wsrc[WP];
#pragma unroll
    for (int i = 0; i < WP; ++i) {
        const int kr = RPP * (WP * w + i) + l / CPR; const int chs = l % CPR;
        int ch;
        if (BN == 128) ch = chs ^ (((kr & 3) << 2) | ((kr >> 2) & 3));
        else ch = chs ^ (((kr & 1) << 2) | ((kr >> 1) & 3));
        wsrc[i] = W + (size_t)kr * ldw + n0 + ch * 8;
    }
    auto stage = [&](int kt, int buf) {
        char* b = lds + buf * BUF;
#pragma unroll
        for (int i = 0; i < 4; ++i) glds16(xsrc[i] + kt * 64, b + (4 * w + i) * 1024);
#pragma unroll
        for (int i = 0; i < WP; ++i) glds16(wsrc[i] + (size_t)kt * 64 * ldw, b + XB + (WP * w + i) * 1024);
    };
    int xoff[2];
#pragma unroll
    for (int m = 0; m < 2; ++m) { const int row = wr * 64 + m * 32 + r; xoff[m] = row * 128; }
    const int xsw = (r >> 1) & 7;
    const int blk = (l >> 4) & 1, q = (l & 15) >> 2, p = l & 3, sg = ((p & 1) << 1) | (p >> 1);
    int woff[NC][2];
#pragma unroll
    for (int c = 0; c < NC; ++c)
#pragma unroll
        for (int t = 0; t < 2; ++t) {
            const int cc = wc * NC + c; const int kr = 8 * h + 4 * t + q;
            const int chunk = 4 * cc + 2 * blk + (sg >> 1);
            int sw;
            if (BN == 128) sw = ((kr & 3) << 2) | ((kr >> 2) & 3);
            else sw = ((kr & 1) << 2) | ((kr >> 1) & 3);
            woff[c][t] = XB + kr * WROW + ((chunk ^ sw) * 16) + 8 * (sg & 1);
        }
    stage(0, 0);
    __syncthreads();
#pragma unroll 1
    for (int kt = 0; kt < 8; ++kt) {
        const int buf = kt & 1;
        if (kt + 1 < 8) stage(kt + 1, buf ^ 1);
        const char* b = lds + buf * BUF;
#pragma unroll
        for (int ks = 0; ks < 4; ++ks) {
            bf16x8 xf[2], wf[NC];
#pragma unroll
            for (int m = 0; m < 2; ++m) xf[m] = *(const bf16x8*)(b + xoff[m] + (((2 * ks + h) ^ xsw) * 16));
#pragma unroll
            for (int c = 0; c < NC; ++c) {
                const s16x4 lo = tr_read(b + woff[c][0] + ks * 16 * WROW), hi = tr_read(b + woff[c][1] + ks * 16 * WROW);
                wf[c] = __builtin_shufflevector(lo, hi, 0, 1, 2, 3, 4, 5, 6, 7);
            }
#pragma unroll
            for (int c = 0; c < NC; ++c)
#pragma unroll
                for (int m = 0; m < 2; ++m) acc[c][m] = MFMA32(wf[c], xf[m], acc[c][m]);
        }
        __syncthreads();
    }
}

template <int BN>
DI void store_tile(const f32x16 (&acc)[BN / 64][2], u16* __restrict__ C, int ldc, int row0, int n0, float scale) {
    constexpr int NC = BN / 64;
    const int tid = threadIdx.x, l = tid & 63, w = tid >> 6, wr = w >> 1, wc = w & 1, h = l >> 5, r = l & 31;
#pragma unroll
    for (int c = 0; c < NC; ++c)
#pragma unroll
        for (int m = 0; m < 2; ++m) {
            u16* dst = C + (size_t)(row0 + wr * 64 + m * 32 + r) * ldc + n0 + (wc * NC + c) * 32 + 8 * h;
#pragma unroll
            for (int s = 0; s < 2; ++s) {
                u32x4 v;
#pragma unroll
                for (int j = 0; j < 4; ++j) v[j] = pk_bf16(acc[c][m][8 * s + 2 * j] * scale, acc[c][m][8 * s + 2 * j + 1] * scale);
                *(u32x4*)(dst + 16 * s) = v;
            }
        }
}

__global__ __launch_bounds__(256, 2) void k_proj(const u16* __restrict__ xa, const u16* __restrict__ wq, const u16* __restrict__ wk, const u16* __restrict__ wv,
                                                 const u16* __restrict__ xb, const u16* __restrict__ wqkv, char* __restrict__ ws) {
    extern __shared__ __attribute__((aligned(16))) char lds[];
    const int b = blockIdx.x;
    if (b < 256) {
        f32x16 acc[2][2] = {};
        if (b < 64) {
            const int rt = b >> 2, ct = b & 3;
            gemm_tile<128>(xa, rt * 128, wq, 512, ct * 128, lds, acc);
            store_tile<128>(acc, (u16*)(ws + WS_QA), 512, rt * 128, ct * 128, QSCALE);
        } else {
            const int bb = b - 64, rt = bb / 12, ct = bb % 12;
            gemm_tile<128>(xb, rt * 128, wqkv, 1536, ct * 128, lds, acc);
            store_tile<128>(acc, (u16*)(ws + WS_QKVB), 1536, rt * 128, ct * 128, 1.0f);
        }
    } else {
        const int t = b - 256;
        const int tid = threadIdx.x, l = tid & 63, w = __builtin_amdgcn_readfirstlane(tid >> 6), h = l >> 5, r = l & 31;
        char* wl = lds + w * 8192;
#pragma unroll
        for (int i = 0; i < 8; ++i) {
            const int k = 128 * w + 16 * i + (l >> 2), ch = l & 3;
            const u16* src = (ch < 2 ? wk : wv) + k * 16 + (ch & 1) * 8;
            glds16(src, wl + i * 1024);
        }
        bf16x8 xf[8];
#pragma unroll
        for (int ks = 0; ks < 8; ++ks) xf[ks] = *(const bf16x8*)(xa + (size_t)(32 * t + r) * DM + 128 * w + 16 * ks + 8 * h);
        asm volatile("s_waitcnt vmcnt(0)" ::: "memory");
        __builtin_amdgcn_sched_barrier(0);
        const int blk = (l >> 4) & 1, q = (l & 15) >> 2, p = l & 3, sg = ((p & 1) << 1) | (p >> 1);
        f32x16 acc = {};
#pragma unroll
        for (int ks = 0; ks < 8; ++ks) {
            const char* a = wl + (16 * ks + 8 * h + q) * 64 + blk * 32 + sg * 8;
            const s16x4 lo = tr_read(a), hi = tr_read(a + 4 * 64);
            acc = MFMA32(__builtin_shufflevector(lo, hi, 0, 1, 2, 3, 4, 5, 6, 7), xf[ks], acc);
        }
        __syncthreads();
        float* red = (float*)lds;
#pragma unroll
        for (int i = 0; i < 16; ++i) red[(w * 16 + i) * 64 + l] = acc[i];
        __syncthreads();
        if (w == 0) {
            float v[16];
#pragma unroll
            for (int i = 0; i < 16; ++i) v[i] = red[i * 64 + l] + red[(16 + i) * 64 + l] + red[(32 + i) * 64 + l] + red[(48 + i) * 64 + l];
            char* kv = ws + WS_KV;
            u32x4 kf;
#pragma unroll
            for (int j = 0; j < 4; ++j) kf[j] = pk_bf16(v[2 * j], v[2 * j + 1]);
            *(u32x4*)(kv + t * 1024 + l * 16) = kf;
            const int g = 2 * t + (r >> 4), hv = (r >> 2) & 1, j = 4 * ((r >> 3) & 1) + (r & 3);
            char* vb = kv + KIMG_BYTES + g * VGRP + hv * 272 + j * 2;
#pragma unroll
            for (int i = 0; i < 8; ++i) {
                const int d = 8 * h + i; const int rho = 8 * ((d >> 2) & 1) + 4 * (d >> 3) + (d & 3);
                *(u16*)(vb + rho * 16) = (u16)(pk_bf16(v[8 + i], 0.f) & 0xffffu);
            }
            if (h == 0) *(u16*)(vb + 16 * 16) = 0x3f80;
        }
    }
}

__global__ __launch_bounds__(256) void k_local(const char* __restrict__ wsr, char* __restrict__ ws) {
    const u16* qkv = (const u16*)(wsr + WS_QKVB);
    const int gid = blockIdx.x * 256 + threadIdx.x, row = gid >> 5, head = gid & 31;
    const u32x4* qp = (const u32x4*)(qkv + (size_t)row * 1536 + head * 16);
    const u32x4 q0 = qp[0], q1 = qp[1];
    float qf[16];
#pragma unroll
    for (int i = 0; i < 4; ++i) { qf[2 * i] = bf_lo(q0[i]); qf[2 * i + 1] = bf_hi(q0[i]); qf[8 + 2 * i] = bf_lo(q1[i]); qf[8 + 2 * i + 1] = bf_hi(q1[i]); }
    float lg[8];
#pragma unroll
    for (int wv = 0; wv < 8; ++wv) {
        const int kr = row - 2 * wv; const int krc = kr < 0 ? 0 : kr;
        const u32x4* kp = (const u32x4*)(qkv + (size_t)krc * 1536 + 512 + head * 16);
        const u32x4 k0 = kp[0], k1 = kp[1];
        float s = 0.f;
#pragma unroll
        for (int i = 0; i < 4; ++i) { s += qf[2 * i] * bf_lo(k0[i]) + qf[2 * i + 1] * bf_hi(k0[i]) + qf[8 + 2 * i] * bf_lo(k1[i]) + qf[8 + 2 * i + 1] * bf_hi(k1[i]); }
        lg[wv] = kr < 0 ? -1e30f : s * QSCALE;
    }
    float m = lg[0];
#pragma unroll
    for (int wv = 1; wv < 8; ++wv) m = fmaxf(m, lg[wv]);
    float lsum = 0.f, o[16];
#pragma unroll
    for (int i = 0; i < 16; ++i) o[i] = 0.f;
#pragma unroll
    for (int wv = 0; wv < 8; ++wv) {
        const float pw = __builtin_amdgcn_exp2f(lg[wv] - m); lsum += pw;
        const int kr = row - 2 * wv; const int krc = kr < 0 ? 0 : kr;
        const u32x4* vp = (const u32x4*)(qkv + (size_t)krc * 1536 + 1024 + head * 16);
        const u32x4 v0 = vp[0], v1 = vp[1];
#pragma unroll
        for (int i = 0; i < 4; ++i) { o[2 * i] += pw * bf_lo(v0[i]); o[2 * i + 1] += pw * bf_hi(v0[i]); o[8 + 2 * i] += pw * bf_lo(v1[i]); o[8 + 2 * i + 1] += pw * bf_hi(v1[i]); }
    }
    const float inv = 1.0f / lsum;
    u32x4 r0, r1;
#pragma unroll
    for (int i = 0; i < 4; ++i) { r0[i] = pk_bf16(o[2 * i] * inv, o[2 * i + 1] * inv); r1[i] = pk_bf16(o[8 + 2 * i] * inv, o[8 + 2 * i + 1] * inv); }
    u32x4* op = (u32x4*)((u16*)(ws + WS_OB) + (size_t)row * 512 + head * 16);
    op[0] = r0; op[1] = r1;
}

__global__ __launch_bounds__(1024, 4) void k_attn(const char* __restrict__ wsr, char* __restrict__ ws) {
    extern __shared__ __attribute__((aligned(16))) char lds[];
    const int tid = threadIdx.x, l = tid & 63, w = __builtin_amdgcn_readfirstlane(tid >> 6), h = l >> 5, r = l & 31;
    const int row = blockIdx.x * 8 + (w & 7), z = w >> 3;
    const char* kv = wsr + WS_KV;
    for (int p = w; p < KV_BYTES / 1024; p += 16) glds16(kv + p * 1024 + l * 16, lds + p * 1024);
    const bf16x8 qf = *(const bf16x8*)((const u16*)(wsr + WS_QA) + (size_t)row * DM + r * 16 + h * 8);
    float* xch = (float*)(lds + KV_BYTES);
    int* flag = (int*)(lds + KV_BYTES + 8 * 9 * 64 * 4);
    float* mx = (float*)(lds + KV_BYTES + 8 * 9 * 64 * 4 + 16);
    if (tid == 0) *flag = 0;
    __syncthreads();
    const char* kb = lds + l * 16;
    const int rr = r < 16 ? r : 16;
    const char* vb = lds + KIMG_BYTES + h * 272 + rr * 16;
    float m;
    {
        const f32x16 zero = {};
        const f32x16 s0 = MFMA32(*(const bf16x8*)kb, qf, zero);
        m = s0[0];
#pragma unroll
        for (int i = 1; i < 16; ++i) m = fmaxf(m, s0[i]);
        auto sw = __builtin_amdgcn_permlane32_swap(__float_as_uint(m), __float_as_uint(m), false, false);
        m = fmaxf(__uint_as_float(sw[0]), __uint_as_float(sw[1])) + MSHIFT;
    }
    float o[8], lsum;
    for (int attempt = 0; attempt < 2; ++attempt) {
        f32x16 negm;
#pragma unroll
        for (int i = 0; i < 16; ++i) negm[i] = -m;
        f32x16 oacc = {};
        const char* kz = kb + z * 32 * 1024; const char* vz = vb + z * 64 * VGRP;
#pragma unroll 4
        for (int t = 0; t < 32; ++t) {
            const bf16x8 kf = *(const bf16x8*)(kz + t * 1024);
            const bf16x8 v0 = *(const bf16x8*)(vz + (2 * t) * VGRP), v1 = *(const bf16x8*)(vz + (2 * t + 1) * VGRP);
            f32x16 s = MFMA32(kf, qf, negm);
            u32x4 p0, p1;
#pragma unroll
            for (int j = 0; j < 4; ++j) {
                p0[j] = __builtin_amdgcn_perm(__float_as_uint(__builtin_amdgcn_exp2f(s[2 * j + 1])), __float_as_uint(__builtin_amdgcn_exp2f(s[2 * j])), 0x07060302u);
                p1[j] = __builtin_amdgcn_perm(__float_as_uint(__builtin_amdgcn_exp2f(s[8 + 2 * j + 1])), __float_as_uint(__builtin_amdgcn_exp2f(s[8 + 2 * j])), 0x07060302u);
            }
            oacc = MFMA32(v0, __builtin_bit_cast(bf16x8, p0), oacc);
            oacc = MFMA32(v1, __builtin_bit_cast(bf16x8, p1), oacc);
        }
        if (z == 1) {
#pragma unroll
            for (int i = 0; i < 9; ++i) xch[((w & 7) * 9 + i) * 64 + l] = oacc[i];
        }
        __syncthreads();
        if (z == 0) {
#pragma unroll
            for (int i = 0; i < 8; ++i) o[i] = oacc[i] + xch[((w & 7) * 9 + i) * 64 + l];
            lsum = oacc[8] + xch[((w & 7) * 9 + 8) * 64 + l];
            bool ok = (lsum > 0x1p-100f) && (lsum < 0x1p126f);
#pragma unroll
            for (int i = 0; i < 8; ++i) ok = ok && (fabsf(o[i]) < 0x1p126f);
            if (attempt == 0 && !__all(ok) && l == 0) *flag = 1;
        }
        __syncthreads();
        if (attempt == 1 || *flag == 0) break;
        {
            const f32x16 zero = {};
            float mm = -INFINITY;
            for (int t = 0; t < 32; ++t) {
                const f32x16 s = MFMA32(*(const bf16x8*)(kz + t * 1024), qf, zero);
#pragma unroll
                for (int i = 0; i < 16; ++i) mm = fmaxf(mm, s[i]);
            }
            auto sw = __builtin_amdgcn_permlane32_swap(__float_as_uint(mm), __float_as_uint(mm), false, false);
            mm = fmaxf(__uint_as_float(sw[0]), __uint_as_float(sw[1]));
            mx[w * 64 + l] = mm;
            __syncthreads();
            m = fmaxf(mx[w * 64 + l], mx[(w ^ 8) * 64 + l]);
        }
    }
    if (z == 0) {
        const float inv = 1.0f / lsum;
        u32x4 ov;
#pragma unroll
        for (int j = 0; j < 4; ++j) ov[j] = pk_bf16(o[2 * j] * inv, o[2 * j + 1] * inv);
        *(u32x4*)((u16*)(ws + WS_OA) + (size_t)row * DM + r * 16 + h * 8) = ov;
    }
}

__global__ __launch_bounds__(256, 2) void k_out(const char* __restrict__ wsr, const u16* __restrict__ woa, const u16* __restrict__ wob, u16* __restrict__ out) {
    extern __shared__ __attribute__((aligned(16))) char lds[];
    const int b = blockIdx.x, br = b >> 7, bb = b & 127, rt = bb >> 3, ct = bb & 7;
    f32x16 acc[1][2] = {};
    gemm_tile<64>((const u16*)(wsr + (br ? WS_OB : WS_OA)), rt * 128, br ? wob : woa, 512, ct * 64, lds, acc);
    store_tile<64>(acc, out + br * 512, 1024, rt * 128, ct * 64, 1.0f);
}

extern "C" void kernel_launch(void* const* d_in, const int* in_sizes, int n_in, void* d_out, int out_size, void* d_ws, size_t ws_size, hipStream_t stream) {
    const u16* xa = (const u16*)d_in[0]; const u16* wq = (const u16*)d_in[1]; const u16* wk = (const u16*)d_in[2]; const u16* wv = (const u16*)d_in[3];
    const u16* woa = (const u16*)d_in[4]; const u16* xb = (const u16*)d_in[5]; const u16* wqkv = (const u16*)d_in[6]; const u16* wob = (const u16*)d_in[7];
    char* ws = (char*)d_ws;
    constexpr int L1 = 2 * (128 * 128 + 64 * 256), L2 = KV_BYTES + 8 * 9 * 64 * 4 + 16 + 16 * 64 * 4, L3 = 2 * (128 * 128 + 64 * 128);
    (void)hipFuncSetAttribute((const void*)k_proj, hipFuncAttributeMaxDynamicSharedMemorySize, L1);
    (void)hipFuncSetAttribute((const void*)k_attn, hipFuncAttributeMaxDynamicSharedMemorySize, L2);
    (void)hipFuncSetAttribute((const void*)k_out, hipFuncAttributeMaxDynamicSharedMemorySize, L3);
    k_proj<<<320, 256, L1, stream>>>(xa, wq, wk, wv, xb, wqkv, ws);
    k_local<<<256, 256, 0, stream>>>(ws, ws);
    k_attn<<<256, 1024, L2, stream>>>(ws, ws);
    k_out<<<256, 256, L3, stream>>>(ws, woa, wob, (u16*)d_out);
}
```

```cpp
#include <hip/hip_runtime.h>
#include <hip/hip_bf16.h>
#include <stdint.h>

typedef short bf16x8 __attribute__((ext_vector_type(8)));
typedef short s16x4 __attribute__((ext_vector_type(4)));
typedef float f32x16 __attribute__((ext_vector_type(16)));
typedef unsigned u32x4 __attribute__((ext_vector_type(4)));
typedef unsigned short u16;
#define LDSP __attribute__((address_space(3)))
#define GLBP __attribute__((address_space(1)))
#define DI __device__ __forceinline__
#define MFMA32(a, b, c) __builtin_amdgcn_mfma_f32_32x32x16_bf16((a), (b), (c), 0, 0, 0)

constexpr int S = 2048, DM = 512;
constexpr float QSCALE = 0.25f * 1.4426950408889634f;
constexpr int KIMG_BYTES = 64 * 1024, VGRP = 544, VIMG_BYTES = 128 * VGRP, KV_BYTES = KIMG_BYTES + VIMG_BYTES;
constexpr float MSHIFT = 64.f;

constexpr size_t WS_QA = 0, WS_KV = 2u << 20, WS_QKVB = 3u << 20, WS_OA = 10u << 20, WS_OB = 12u << 20;

typedef __bf16 bf16x2_t __attribute__((ext_vector_type(2)));
typedef float f32x2_t __attribute__((ext_vector_type(2)));
DI unsigned pk_bf16(float lo, float hi) { f32x2_t v = {lo, hi}; return __builtin_bit_cast(unsigned, __builtin_convertvector(v, bf16x2_t)); }
DI float bf_lo(unsigned u) { return __uint_as_float(u << 16); }
DI float bf_hi(unsigned u) { return __uint_as_float(u & 0xffff0000u); }
DI void glds16(const void* g, void* l) { __builtin_amdgcn_global_load_lds((const GLBP unsigned*)g, (LDSP unsigned*)l, 16, 0, 0); }
typedef short v4i16_t __attribute__((ext_vector_type(4)));
DI s16x4 tr_read(const char* p) { return __builtin_bit_cast(s16x4, __builtin_amdgcn_ds_read_tr16_b64_v4i16((LDSP v4i16_t*)p)); }
DI int crow(int r, int hi) { return (r & 3) + 8 * (r >> 2) + 4 * hi; }

#define STAMP(kid, s) do {} while (0)
template <int BN>
DI void gemm_tile(const u16* __restrict__ X, int row0, const u16* __restrict__ W, int ldw, int n0, char* lds, f32x16 (&acc)[BN / 64][2]) {
    constexpr int NC = BN / 64;
    constexpr int XB = 128 * 128;
    constexpr int WROW = BN * 2;
    constexpr int WB = 64 * WROW;
    constexpr int BUF = XB + WB;
    const int tid = threadIdx.x, l = tid & 63, w = __builtin_amdgcn_readfirstlane(tid >> 6);
    const int wr = w >> 1, wc = w & 1, h = l >> 5, r = l & 31;
    const u16* xsrc[4];
#pragma unroll
    for (int i = 0; i < 4; ++i) { const int row = 8 * (4 * w + i) + (l >> 3); const int c = (l & 7) ^ ((row >> 1) & 7); xsrc[i] = X + (size_t)(row0 + row) * DM + c * 8; }
    constexpr int WP = WB / 1024 / 4;
    constexpr int RPP = 1024 / WROW;
    constexpr int CPR = WROW / 16;
    const u16* wsrc[WP];
#pragma unroll
    for (int i = 0; i < WP; ++i) {
        const int kr = RPP * (WP * w + i) + l / CPR; const int chs = l % CPR;
        int ch;
        if (BN == 128) ch = chs ^ (((kr & 3) << 2) | ((kr >> 2) & 3));
        else ch = chs ^ (((kr & 1) << 2) | ((kr >> 1) & 3));
        wsrc[i] = W + (size_t)kr * ldw + n0 + ch * 8;
    }
    auto stage = [&](int kt, int buf) {
        char* b = lds + buf * BUF;
#pragma unroll
        for (int i = 0; i < 4; ++i) glds16(xsrc[i] + kt * 64, b + (4 * w + i) * 1024);
#pragma unroll
        for (int i = 0; i < WP; ++i) glds16(wsrc[i] + (size_t)kt * 64 * ldw, b + XB + (WP * w + i) * 1024);
    };
    int xoff[2];
#pragma unroll
    for (int m = 0; m < 2; ++m) { const int row = wr * 64 + m * 32 + r; xoff[m] = row * 128; }
    const int xsw = (r >> 1) & 7;
    const int blk = (l >> 4) & 1, q = (l & 15) >> 2, p = l & 3, sg = ((p & 1) << 1) | (p >> 1);
    int woff[NC][2];
#pragma unroll
    for (int c = 0; c < NC; ++c)
#pragma unroll
        for (int t = 0; t < 2; ++t) {
            const int cc = wc * NC + c; const int kr = 8 * h + 4 * t + q;
            const int chunk = 4 * cc + 2 * blk + (sg >> 1);
            int sw;
            if (BN == 128) sw = ((kr & 3) << 2) | ((kr >> 2) & 3);
            else sw = ((kr & 1) << 2) | ((kr >> 1) & 3);
            woff[c][t] = XB + kr * WROW + ((chunk ^ sw) * 16) + 8 * (sg & 1);
        }
    stage(0, 0);
    __syncthreads();
#pragma unroll 1
    for (int kt = 0; kt < 8; ++kt) {
        const int buf = kt & 1;
        if (kt + 1 < 8) stage(kt + 1, buf ^ 1);
        const char* b = lds + buf * BUF;
#pragma unroll
        for (int ks = 0; ks < 4; ++ks) {
            bf16x8 xf[2], wf[NC];
#pragma unroll
            for (int m = 0; m < 2; ++m) xf[m] = *(const bf16x8*)(b + xoff[m] + (((2 * ks + h) ^ xsw) * 16));
#pragma unroll
            for (int c = 0; c < NC; ++c) {
                const s16x4 lo = tr_read(b + woff[c][0] + ks * 16 * WROW), hi = tr_read(b + woff[c][1] + ks * 16 * WROW);
                wf[c] = __builtin_shufflevector(lo, hi, 0, 1, 2, 3, 4, 5, 6, 7);
            }
#pragma unroll
            for (int c = 0; c < NC; ++c)
#pragma unroll
                for (int m = 0; m < 2; ++m) acc[c][m] = MFMA32(wf[c], xf[m], acc[c][m]);
        }
        __syncthreads();
    }
}

template <int BN>
DI void store_tile(const f32x16 (&acc)[BN / 64][2], u16* __restrict__ C, int ldc, int row0, int n0, float scale) {
    constexpr int NC = BN / 64;
    const int tid = threadIdx.x, l = tid & 63, w = tid >> 6, wr = w >> 1, wc = w & 1, h = l >> 5, r = l & 31;
#pragma unroll
    for (int c = 0; c < NC; ++c)
#pragma unroll
        for (int m = 0; m < 2; ++m) {
            u16* dst = C + (size_t)(row0 + wr * 64 + m * 32 + r) * ldc + n0 + (wc * NC + c) * 32 + 8 * h;
#pragma unroll
            for (int s = 0; s < 2; ++s) {
                u32x4 v;
#pragma unroll
                for (int j = 0; j < 4; ++j) v[j] = pk_bf16(acc[c][m][8 * s + 2 * j] * scale, acc[c][m][8 * s + 2 * j + 1] * scale);
                *(u32x4*)(dst + 16 * s) = v;
            }
        }
}

__global__ __launch_bounds__(256, 2) void k_proj(const u16* __restrict__ xa, const u16* __restrict__ wq, const u16* __restrict__ wk, const u16* __restrict__ wv,
                                                 const u16* __restrict__ xb, const u16* __restrict__ wqkv, char* __restrict__ ws) {
    extern __shared__ __attribute__((aligned(16))) char lds[];
    const int b = blockIdx.x;
    STAMP(0, 0);
    if (b < 256) {
        f32x16 acc[2][2] = {};
        if (b < 64) {
            const int rt = b >> 2, ct = b & 3;
            gemm_tile<128>(xa, rt * 128, wq, 512, ct * 128, lds, acc);
            store_tile<128>(acc, (u16*)(ws + WS_QA), 512, rt * 128, ct * 128, QSCALE);
        } else {
            const int bb = b - 64, rt = bb / 12, ct = bb % 12;
            gemm_tile<128>(xb, rt * 128, wqkv, 1536, ct * 128, lds, acc);
            store_tile<128>(acc, (u16*)(ws + WS_QKVB), 1536, rt * 128, ct * 128, 1.0f);
        }
    } else {
        const int t = b - 256;
        const int tid = threadIdx.x, l = tid & 63, w = __builtin_amdgcn_readfirstlane(tid >> 6), h = l >> 5, r = l & 31;
        char* wl = lds + w * 8192;
#pragma unroll
        for (int i = 0; i < 8; ++i) {
            const int k = 128 * w + 16 * i + (l >> 2), ch = l & 3;
            const u16* src = (ch < 2 ? wk : wv) + k * 16 + (ch & 1) * 8;
            glds16(src, wl + i * 1024);
        }
        bf16x8 xf[8];
#pragma unroll
        for (int ks = 0; ks < 8; ++ks) xf[ks] = *(const bf16x8*)(xa + (size_t)(32 * t + r) * DM + 128 * w + 16 * ks + 8 * h);
        asm volatile("s_waitcnt vmcnt(0)" ::: "memory");
        __builtin_amdgcn_sched_barrier(0);
        const int blk = (l >> 4) & 1, q = (l & 15) >> 2, p = l & 3, sg = ((p & 1) << 1) | (p >> 1);
        f32x16 acc = {};
#pragma unroll
        for (int ks = 0; ks < 8; ++ks) {
            const char* a = wl + (16 * ks + 8 * h + q) * 64 + blk * 32 + sg * 8;
            const s16x4 lo = tr_read(a), hi = tr_read(a + 4 * 64);
            acc = MFMA32(__builtin_shufflevector(lo, hi, 0, 1, 2, 3, 4, 5, 6, 7), xf[ks], acc);
        }
        __syncthreads();
        float* red = (float*)lds;
#pragma unroll
        for (int i = 0; i < 16; ++i) red[(w * 16 + i) * 64 + l] = acc[i];
        __syncthreads();
        if (w == 0) {
            float v[16];
#pragma unroll
            for (int i = 0; i < 16; ++i) v[i] = red[i * 64 + l] + red[(16 + i) * 64 + l] + red[(32 + i) * 64 + l] + red[(48 + i) * 64 + l];
            char* kv = ws + WS_KV;
            u32x4 kf;
#pragma unroll
            for (int j = 0; j < 4; ++j) kf[j] = pk_bf16(v[2 * j], v[2 * j + 1]);
            *(u32x4*)(kv + t * 1024 + l * 16) = kf;
            const int g = 2 * t + (r >> 4), hv = (r >> 2) & 1, j = 4 * ((r >> 3) & 1) + (r & 3);
            char* vb = kv + KIMG_BYTES + g * VGRP + hv * 272 + j * 2;
#pragma unroll
            for (int i = 0; i < 8; ++i) {
                const int d = 8 * h + i; const int rho = 8 * ((d >> 2) & 1) + 4 * (d >> 3) + (d & 3);
                *(u16*)(vb + rho * 16) = (u16)(pk_bf16(v[8 + i], 0.f) & 0xffffu);
            }
            if (h == 0) *(u16*)(vb + 16 * 16) = 0x3f80;
        }
    }
    STAMP(0, 1);
}

__global__ __launch_bounds__(256) void k_local(const char* __restrict__ wsr, char* __restrict__ ws) {
    STAMP(1, 0);
    const u16* qkv = (const u16*)(wsr + WS_QKVB);
    const int gid = blockIdx.x * 256 + threadIdx.x, row = gid >> 5, head = gid & 31;
    const u32x4* qp = (const u32x4*)(qkv + (size_t)row * 1536 + head * 16);
    const u32x4 q0 = qp[0], q1 = qp[1];
    float qf[16];
#pragma unroll
    for (int i = 0; i < 4; ++i) { qf[2 * i] = bf_lo(q0[i]); qf[2 * i + 1] = bf_hi(q0[i]); qf[8 + 2 * i] = bf_lo(q1[i]); qf[8 + 2 * i + 1] = bf_hi(q1[i]); }
    float lg[8];
#pragma unroll
    for (int wv = 0; wv < 8; ++wv) {
        const int kr = row - 2 * wv; const int krc = kr < 0 ? 0 : kr;
        const u32x4* kp = (const u32x4*)(qkv + (size_t)krc * 1536 + 512 + head * 16);
        const u32x4 k0 = kp[0], k1 = kp[1];
        float s = 0.f;
#pragma unroll
        for (int i = 0; i < 4; ++i) { s += qf[2 * i] * bf_lo(k0[i]) + qf[2 * i + 1] * bf_hi(k0[i]) + qf[8 + 2 * i] * bf_lo(k1[i]) + qf[8 + 2 * i + 1] * bf_hi(k1[i]); }
        lg[wv] = kr < 0 ? -1e30f : s * QSCALE;
    }
    float m = lg[0];
#pragma unroll
    for (int wv = 1; wv < 8; ++wv) m = fmaxf(m, lg[wv]);
    float lsum = 0.f, o[16];
#pragma unroll
    for (int i = 0; i < 16; ++i) o[i] = 0.f;
#pragma unroll
    for (int wv = 0; wv < 8; ++wv) {
        const float pw = __builtin_amdgcn_exp2f(lg[wv] - m); lsum += pw;
        const int kr = row - 2 * wv; const int krc = kr < 0 ? 0 : kr;
        const u32x4* vp = (const u32x4*)(qkv + (size_t)krc * 1536 + 1024 + head * 16);
        const u32x4 v0 = vp[0], v1 = vp[1];
#pragma unroll
        for (int i = 0; i < 4; ++i) { o[2 * i] += pw * bf_lo(v0[i]); o[2 * i + 1] += pw * bf_hi(v0[i]); o[8 + 2 * i] += pw * bf_lo(v1[i]); o[8 + 2 * i + 1] += pw * bf_hi(v1[i]); }
    }
    const float inv = 1.0f / lsum;
    u32x4 r0, r1;
#pragma unroll
    for (int i = 0; i < 4; ++i) { r0[i] = pk_bf16(o[2 * i] * inv, o[2 * i + 1] * inv); r1[i] = pk_bf16(o[8 + 2 * i] * inv, o[8 + 2 * i + 1] * inv); }
    u32x4* op = (u32x4*)((u16*)(ws + WS_OB) + (size_t)row * 512 + head * 16);
    op[0] = r0; op[1] = r1;
    STAMP(1, 1);
}

__global__ __launch_bounds__(1024, 4) void k_attn(const char* __restrict__ wsr, char* __restrict__ ws) {
    extern __shared__ __attribute__((aligned(16))) char lds[];
    const int tid = threadIdx.x, l = tid & 63, w = __builtin_amdgcn_readfirstlane(tid >> 6), h = l >> 5, r = l & 31;
    const int row = blockIdx.x * 8 + (w & 7), z = w >> 3;
    STAMP(2, 0);
    const char* kv = wsr + WS_KV;
    for (int p = w; p < KV_BYTES / 1024; p += 16) glds16(kv + p * 1024 + l * 16, lds + p * 1024);
    const bf16x8 qf = *(const bf16x8*)((const u16*)(wsr + WS_QA) + (size_t)row * DM + r * 16 + h * 8);
    float* xch = (float*)(lds + KV_BYTES);
    int* flag = (int*)(lds + KV_BYTES + 8 * 9 * 64 * 4);
    float* mx = (float*)(lds + KV_BYTES + 8 * 9 * 64 * 4 + 16);
    if (tid == 0) *flag = 0;
    __syncthreads();
    STAMP(2, 1);
    const char* kb = lds + l * 16;
    const int rr = r < 16 ? r : 16;
    const char* vb = lds + KIMG_BYTES + h * 272 + rr * 16;
    float m;
    {
        const f32x16 zero = {};
        const f32x16 s0 = MFMA32(*(const bf16x8*)kb, qf, zero);
        m = s0[0];
#pragma unroll
        for (int i = 1; i < 16; ++i) m = fmaxf(m, s0[i]);
        auto sw = __builtin_amdgcn_permlane32_swap(__float_as_uint(m), __float_as_uint(m), false, false);
        m = fmaxf(__uint_as_float(sw[0]), __uint_as_float(sw[1])) + MSHIFT;
    }
    float o[8], lsum;
    for (int attempt = 0; attempt < 2; ++attempt) {
        f32x16 negm;
#pragma unroll
        for (int i = 0; i < 16; ++i) negm[i] = -m;
        f32x16 oacc = {};
        const char* kz = kb + z * 32 * 1024; const char* vz = vb + z * 64 * VGRP;
#pragma unroll 4
        for (int t = 0; t < 32; ++t) {
            const bf16x8 kf = *(const bf16x8*)(kz + t * 1024);
            const bf16x8 v0 = *(const bf16x8*)(vz + (2 * t) * VGRP), v1 = *(const bf16x8*)(vz + (2 * t + 1) * VGRP);
            f32x16 s = MFMA32(kf, qf, negm);
            u32x4 p0, p1;
#pragma unroll
            for (int j = 0; j < 4; ++j) {
                p0[j] = __builtin_amdgcn_perm(__float_as_uint(__builtin_amdgcn_exp2f(s[2 * j + 1])), __float_as_uint(__builtin_amdgcn_exp2f(s[2 * j])), 0x07060302u);
                p1[j] = __builtin_amdgcn_perm(__float_as_uint(__builtin_amdgcn_exp2f(s[8 + 2 * j + 1])), __float_as_uint(__builtin_amdgcn_exp2f(s[8 + 2 * j])), 0x07060302u);
            }
            oacc = MFMA32(v0, __builtin_bit_cast(bf16x8, p0), oacc);
            oacc = MFMA32(v1, __builtin_bit_cast(bf16x8, p1), oacc);
        }
        asm volatile("" :: "v"(oacc));
        STAMP(2, 2);
        if (z == 1) {
#pragma unroll
            for (int i = 0; i < 9; ++i) xch[((w & 7) * 9 + i) * 64 + l] = oacc[i];
        }
        __syncthreads();
        if (z == 0) {
#pragma unroll
            for (int i = 0; i < 8; ++i) o[i] = oacc[i] + xch[((w & 7) * 9 + i) * 64 + l];
            lsum = oacc[8] + xch[((w & 7) * 9 + 8) * 64 + l];
            bool ok = (lsum > 0x1p-100f) && (lsum < 0x1p126f);
#pragma unroll
            for (int i = 0; i < 8; ++i) ok = ok && (fabsf(o[i]) < 0x1p126f);
            if (attempt == 0 && !__all(ok) && l == 0) *flag = 1;
        }
        __syncthreads();
        if (attempt == 1 || *flag == 0) break;
        {
            const f32x16 zero = {};
            float mm = -INFINITY;
            for (int t = 0; t < 32; ++t) {
                const f32x16 s = MFMA32(*(const bf16x8*)(kz + t * 1024), qf, zero);
#pragma unroll
                for (int i = 0; i < 16; ++i) mm = fmaxf(mm, s[i]);
            }
            auto sw = __builtin_amdgcn_permlane32_swap(__float_as_uint(mm), __float_as_uint(mm), false, false);
            mm = fmaxf(__uint_as_float(sw[0]), __uint_as_float(sw[1]));
            mx[w * 64 + l] = mm;
            __syncthreads();
            m = fmaxf(mx[w * 64 + l], mx[(w ^ 8) * 64 + l]);
        }
    }
    if (z == 0) {
        const float inv = 1.0f / lsum;
        u32x4 ov;
#pragma unroll
        for (int j = 0; j < 4; ++j) ov[j] = pk_bf16(o[2 * j] * inv, o[2 * j + 1] * inv);
        *(u32x4*)((u16*)(ws + WS_OA) + (size_t)row * DM + r * 16 + h * 8) = ov;
    }
    STAMP(2, 3);
}

__global__ __launch_bounds__(256, 2) void k_out(char* __restrict__ ws, const char* __restrict__ wsr, const u16* __restrict__ woa, const u16* __restrict__ wob, u16* __restrict__ out) {
    extern __shared__ __attribute__((aligned(16))) char lds[];
    STAMP(3, 0);
    const int b = blockIdx.x, br = b >> 7, bb = b & 127, rt = bb >> 3, ct = bb & 7;
    f32x16 acc[1][2] = {};
    gemm_tile<64>((const u16*)(wsr + (br ? WS_OB : WS_OA)), rt * 128, br ? wob : woa, 512, ct * 64, lds, acc);
    store_tile<64>(acc, out + br * 512, 1024, rt * 128, ct * 64, 1.0f);
    STAMP(3, 1);
}

extern "C" void kernel_launch(void* const* d_in, const int* in_sizes, int n_in, void* d_out, int out_size, void* d_ws, size_t ws_size, hipStream_t stream) {
    const u16* xa = (const u16*)d_in[0]; const u16* wq = (const u16*)d_in[1]; const u16* wk = (const u16*)d_in[2]; const u16* wv = (const u16*)d_in[3];
    const u16* woa = (const u16*)d_in[4]; const u16* xb = (const u16*)d_in[5]; const u16* wqkv = (const u16*)d_in[6]; const u16* wob = (const u16*)d_in[7];
    char* ws = (char*)d_ws;
    constexpr int L1 = 2 * (128 * 128 + 64 * 256), L2 = KV_BYTES + 8 * 9 * 64 * 4 + 16 + 16 * 64 * 4, L3 = 2 * (128 * 128 + 64 * 128);
    (void)hipFuncSetAttribute((const void*)k_proj, hipFuncAttributeMaxDynamicSharedMemorySize, L1);
    (void)hipFuncSetAttribute((const void*)k_attn, hipFuncAttributeMaxDynamicSharedMemorySize, L2);
    (void)hipFuncSetAttribute((const void*)k_out, hipFuncAttributeMaxDynamicSharedMemorySize, L3);
    k_proj<<<320, 256, L1, stream>>>(xa, wq, wk, wv, xb, wqkv, ws);
    k_local<<<256, 256, 0, stream>>>(ws, ws);
    k_attn<<<256, 1024, L2, stream>>>(ws, ws);
    k_out<<<256, 256, L3, stream>>>(ws, ws, woa, wob, (u16*)d_out);
}
```

```cpp
    "s_mov_b32 s91, 0\n\t"
    "ds_read_b128 %[k], %[ka]\n\t"
    "ds_read_b128 %[v0], %[va]\n\t"
    "v_mov_b32_e32 v252, 0\n\t"
    "v_mov_b32_e32 v253, 0\n\t"
    "v_mov_b32_e32 v254, 0\n\t"
    "v_mov_b32_e32 v255, 0\n\t"
    "s_waitcnt lgkmcnt(0)\n\t"
    "v_mfma_f32_32x32x16_bf16 v[216:231], %[k], %[qa], %[na]\n\t"
    "s_nop 11\n\t"
    "1:\n\t"
    "v_mfma_f32_32x32x16_bf16 v[232:247], %[k], %[qb], %[nb]\n\t"
    "ds_read_b128 %[k], %[ka] offset:1024\n\t"
    "v_exp_f32_e32 v248, v217\n\t"
    "v_exp_f32_e32 v216, v216\n\t"
    "v_exp_f32_e32 v249, v219\n\t"
    "v_exp_f32_e32 v218, v218\n\t"
    "v_mov_b32_sdwa v248, v216 dst_sel:WORD_0 dst_unused:UNUSED_PRESERVE src0_sel:WORD_1\n\t"
    "v_exp_f32_e32 v250, v221\n\t"
    "v_mfma_f32_32x32x16_bf16 %[ob], %[v1], v[252:255], %[ob]\n\t"
    "ds_read_b128 %[v1], %[va] offset:544\n\t"
    "v_exp_f32_e32 v220, v220\n\t"
    "v_mov_b32_sdwa v249, v218 dst_sel:WORD_0 dst_unused:UNUSED_PRESERVE src0_sel:WORD_1\n\t"
    "v_exp_f32_e32 v251, v223\n\t"
    "v_exp_f32_e32 v222, v222\n\t"
    "v_mov_b32_sdwa v250, v220 dst_sel:WORD_0 dst_unused:UNUSED_PRESERVE src0_sel:WORD_1\n\t"
    "v_exp_f32_e32 v252, v225\n\t"
    "v_mov_b32_sdwa v251, v222 dst_sel:WORD_0 dst_unused:UNUSED_PRESERVE src0_sel:WORD_1\n\t"
    "v_exp_f32_e32 v224, v224\n\t"
    "v_exp_f32_e32 v253, v227\n\t"
    "s_waitcnt lgkmcnt(2)\n\t"
    "v_mfma_f32_32x32x16_bf16 %[oa], %[v0], v[248:251], %[oa]\n\t"
    "v_exp_f32_e32 v226, v226\n\t"
    "v_mov_b32_sdwa v252, v224 dst_sel:WORD_0 dst_unused:UNUSED_PRESERVE src0_sel:WORD_1\n\t"
    "v_exp_f32_e32 v254, v229\n\t"
    "v_exp_f32_e32 v228, v228\n\t"
    "v_mov_b32_sdwa v253, v226 dst_sel:WORD_0 dst_unused:UNUSED_PRESERVE src0_sel:WORD_1\n\t"
    "v_exp_f32_e32 v255, v231\n\t"
    "v_exp_f32_e32 v230, v230\n\t"
    "v_mov_b32_sdwa v254, v228 dst_sel:WORD_0 dst_unused:UNUSED_PRESERVE src0_sel:WORD_1\n\t"
    "v_mov_b32_sdwa v255, v230 dst_sel:WORD_0 dst_unused:UNUSED_PRESERVE src0_sel:WORD_1\n\t"
    "s_waitcnt lgkmcnt(1)\n\t"
    "v_mfma_f32_32x32x16_bf16 v[216:231], %[k], %[qa], %[na]\n\t"
    "v_exp_f32_e32 v248, v233\n\t"
    "v_exp_f32_e32 v232, v232\n\t"
    "v_exp_f32_e32 v249, v235\n\t"
    "v_exp_f32_e32 v234, v234\n\t"
    "v_mov_b32_sdwa v248, v232 dst_sel:WORD_0 dst_unused:UNUSED_PRESERVE src0_sel:WORD_1\n\t"
    "v_exp_f32_e32 v250, v237\n\t"
    "s_waitcnt lgkmcnt(0)\n\t"
    "v_mfma_f32_32x32x16_bf16 %[oa], %[v1], v[252:255], %[oa]\n\t"
    "v_exp_f32_e32 v236, v236\n\t"
    "v_mov_b32_sdwa v249, v234 dst_sel:WORD_0 dst_unused:UNUSED_PRESERVE src0_sel:WORD_1\n\t"
    "v_exp_f32_e32 v251, v239\n\t"
    "v_exp_f32_e32 v238, v238\n\t"
    "v_mov_b32_sdwa v250, v236 dst_sel:WORD_0 dst_unused:UNUSED_PRESERVE src0_sel:WORD_1\n\t"
    "v_exp_f32_e32 v252, v241\n\t"
    "v_mov_b32_sdwa v251, v238 dst_sel:WORD_0 dst_unused:UNUSED_PRESERVE src0_sel:WORD_1\n\t"
    "v_exp_f32_e32 v240, v240\n\t"
    "v_exp_f32_e32 v253, v243\n\t"
    "v_mfma_f32_32x32x16_bf16 %[ob], %[v0], v[248:251], %[ob]\n\t"
    "ds_read_b128 %[v0], %[va] offset:1088\n\t"
    "v_exp_f32_e32 v242, v242\n\t"
    "v_mov_b32_sdwa v252, v240 dst_sel:WORD_0 dst_unused:UNUSED_PRESERVE src0_sel:WORD_1\n\t"
    "v_exp_f32_e32 v254, v245\n\t"
    "v_exp_f32_e32 v244, v244\n\t"
    "v_mov_b32_sdwa v253, v242 dst_sel:WORD_0 dst_unused:UNUSED_PRESERVE src0_sel:WORD_1\n\t"
    "v_exp_f32_e32 v255, v247\n\t"
    "v_exp_f32_e32 v246, v246\n\t"
    "v_mov_b32_sdwa v254, v244 dst_sel:WORD_0 dst_unused:UNUSED_PRESERVE src0_sel:WORD_1\n\t"
    "v_mov_b32_sdwa v255, v246 dst_sel:WORD_0 dst_unused:UNUSED_PRESERVE src0_sel:WORD_1\n\t"
    "v_mfma_f32_32x32x16_bf16 v[232:247], %[k], %[qb], %[nb]\n\t"
    "ds_read_b128 %[k], %[ka] offset:2048\n\t"
    "v_exp_f32_e32 v248, v217\n\t"
    "v_exp_f32_e32 v216, v216\n\t"
    "v_exp_f32_e32 v249, v219\n\t"
    "v_exp_f32_e32 v218, v218\n\t"
    "v_mov_b32_sdwa v248, v216 dst_sel:WORD_0 dst_unused:UNUSED_PRESERVE src0_sel:WORD_1\n\t"
    "v_exp_f32_e32 v250, v221\n\t"
    "v_mfma_f32_32x32x16_bf16 %[ob], %[v1], v[252:255], %[ob]\n\t"
    "ds_read_b128 %[v1], %[va] offset:1632\n\t"
    "v_exp_f32_e32 v220, v220\n\t"
    "v_mov_b32_sdwa v249, v218 dst_sel:WORD_0 dst_unused:UNUSED_PRESERVE src0_sel:WORD_1\n\t"
    "v_exp_f32_e32 v251, v223\n\t"
    "v_exp_f32_e32 v222, v222\n\t"
    "v_mov_b32_sdwa v250, v220 dst_sel:WORD_0 dst_unused:UNUSED_PRESERVE src0_sel:WORD_1\n\t"
    "v_exp_f32_e32 v252, v225\n\t"
    "v_mov_b32_sdwa v251, v222 dst_sel:WORD_0 dst_unused:UNUSED_PRESERVE src0_sel:WORD_1\n\t"
    "v_exp_f32_e32 v224, v224\n\t"
    "v_exp_f32_e32 v253, v227\n\t"
    "s_waitcnt lgkmcnt(2)\n\t"
    "v_mfma_f32_32x32x16_bf16 %[oa], %[v0], v[248:251], %[oa]\n\t"
    "v_exp_f32_e32 v226, v226\n\t"
    "v_mov_b32_sdwa v252, v224 dst_sel:WORD_0 dst_unused:UNUSED_PRESERVE src0_sel:WORD_1\n\t"
    "v_exp_f32_e32 v254, v229\n\t"
    "v_exp_f32_e32 v228, v228\n\t"
    "v_mov_b32_sdwa v253, v226 dst_sel:WORD_0 dst_unused:UNUSED_PRESERVE src0_sel:WORD_1\n\t"
    "v_exp_f32_e32 v255, v231\n\t"
    "v_exp_f32_e32 v230, v230\n\t"
    "v_mov_b32_sdwa v254, v228 dst_sel:WORD_0 dst_unused:UNUSED_PRESERVE src0_sel:WORD_1\n\t"
    "v_mov_b32_sdwa v255, v230 dst_sel:WORD_0 dst_unused:UNUSED_PRESERVE src0_sel:WORD_1\n\t"
    "s_waitcnt lgkmcnt(1)\n\t"
    "v_mfma_f32_32x32x16_bf16 v[216:231], %[k], %[qa], %[na]\n\t"
    "v_exp_f32_e32 v248, v233\n\t"
    "v_exp_f32_e32 v232, v232\n\t"
    "v_exp_f32_e32 v249, v235\n\t"
    "v_exp_f32_e32 v234, v234\n\t"
    "v_mov_b32_sdwa v248, v232 dst_sel:WORD_0 dst_unused:UNUSED_PRESERVE src0_sel:WORD_1\n\t"
    "v_exp_f32_e32 v250, v237\n\t"
    "s_waitcnt lgkmcnt(0)\n\t"
    "v_mfma_f32_32x32x16_bf16 %[oa], %[v1], v[252:255], %[oa]\n\t"
    "v_exp_f32_e32 v236, v236\n\t"
    "v_mov_b32_sdwa v249, v234 dst_sel:WORD_0 dst_unused:UNUSED_PRESERVE src0_sel:WORD_1\n\t"
    "v_exp_f32_e32 v251, v239\n\t"
    "v_exp_f32_e32 v238, v238\n\t"
    "v_mov_b32_sdwa v250, v236 dst_sel:WORD_0 dst_unused:UNUSED_PRESERVE src0_sel:WORD_1\n\t"
    "v_exp_f32_e32 v252, v241\n\t"
    "v_mov_b32_sdwa v251, v238 dst_sel:WORD_0 dst_unused:UNUSED_PRESERVE src0_sel:WORD_1\n\t"
    "v_exp_f32_e32 v240, v240\n\t"
    "v_exp_f32_e32 v253, v243\n\t"
    "v_mfma_f32_32x32x16_bf16 %[ob], %[v0], v[248:251], %[ob]\n\t"
    "ds_read_b128 %[v0], %[va] offset:2176\n\t"
    "v_exp_f32_e32 v242, v242\n\t"
    "v_mov_b32_sdwa v252, v240 dst_sel:WORD_0 dst_unused:UNUSED_PRESERVE src0_sel:WORD_1\n\t"
    "v_exp_f32_e32 v254, v245\n\t"
    "v_exp_f32_e32 v244, v244\n\t"
    "v_mov_b32_sdwa v253, v242 dst_sel:WORD_0 dst_unused:UNUSED_PRESERVE src0_sel:WORD_1\n\t"
    "v_exp_f32_e32 v255, v247\n\t"
    "v_exp_f32_e32 v246, v246\n\t"
    "v_mov_b32_sdwa v254, v244 dst_sel:WORD_0 dst_unused:UNUSED_PRESERVE src0_sel:WORD_1\n\t"
    "v_mov_b32_sdwa v255, v246 dst_sel:WORD_0 dst_unused:UNUSED_PRESERVE src0_sel:WORD_1\n\t"
    "v_mfma_f32_32x32x16_bf16 v[232:247], %[k], %[qb], %[nb]\n\t"
    "ds_read_b128 %[k], %[ka] offset:3072\n\t"
    "v_exp_f32_e32 v248, v217\n\t"
    "v_exp_f32_e32 v216, v216\n\t"
    "v_exp_f32_e32 v249, v219\n\t"
    "v_exp_f32_e32 v218, v218\n\t"
    "v_mov_b32_sdwa v248, v216 dst_sel:WORD_0 dst_unused:UNUSED_PRESERVE src0_sel:WORD_1\n\t"
    "v_exp_f32_e32 v250, v221\n\t"
    "v_mfma_f32_32x32x16_bf16 %[ob], %[v1], v[252:255], %[ob]\n\t"
    "ds_read_b128 %[v1], %[va] offset:2720\n\t"
    "v_exp_f32_e32 v220, v220\n\t"
    "v_mov_b32_sdwa v249, v218 dst_sel:WORD_0 dst_unused:UNUSED_PRESERVE src0_sel:WORD_1\n\t"
    "v_exp_f32_e32 v251, v223\n\t"
    "v_exp_f32_e32 v222, v222\n\t"
    "v_mov_b32_sdwa v250, v220 dst_sel:WORD_0 dst_unused:UNUSED_PRESERVE src0_sel:WORD_1\n\t"
    "v_exp_f32_e32 v252, v225\n\t"
    "v_mov_b32_sdwa v251, v222 dst_sel:WORD_0 dst_unused:UNUSED_PRESERVE src0_sel:WORD_1\n\t"
    "v_exp_f32_e32 v224, v224\n\t"
    "v_exp_f32_e32 v253, v227\n\t"
    "s_waitcnt lgkmcnt(2)\n\t"
    "v_mfma_f32_32x32x16_bf16 %[oa], %[v0], v[248:251], %[oa]\n\t"
    "v_exp_f32_e32 v226, v226\n\t"
    "v_mov_b32_sdwa v252, v224 dst_sel:WORD_0 dst_unused:UNUSED_PRESERVE src0_sel:WORD_1\n\t"
    "v_exp_f32_e32 v254, v229\n\t"
    "v_exp_f32_e32 v228, v228\n\t"
    "v_mov_b32_sdwa v253, v226 dst_sel:WORD_0 dst_unused:UNUSED_PRESERVE src0_sel:WORD_1\n\t"
    "v_exp_f32_e32 v255, v231\n\t"
    "v_exp_f32_e32 v230, v230\n\t"
    "v_mov_b32_sdwa v254, v228 dst_sel:WORD_0 dst_unused:UNUSED_PRESERVE src0_sel:WORD_1\n\t"
    "v_mov_b32_sdwa v255, v230 dst_sel:WORD_0 dst_unused:UNUSED_PRESERVE src0_sel:WORD_1\n\t"
    "s_waitcnt lgkmcnt(1)\n\t"
    "v_mfma_f32_32x32x16_bf16 v[216:231], %[k], %[qa], %[na]\n\t"
    "v_exp_f32_e32 v248, v233\n\t"
    "v_exp_f32_e32 v232, v232\n\t"
    "v_exp_f32_e32 v249, v235\n\t"
    "v_exp_f32_e32 v234, v234\n\t"
    "v_mov_b32_sdwa v248, v232 dst_sel:WORD_0 dst_unused:UNUSED_PRESERVE src0_sel:WORD_1\n\t"
    "v_exp_f32_e32 v250, v237\n\t"
    "s_waitcnt lgkmcnt(0)\n\t"
    "v_mfma_f32_32x32x16_bf16 %[oa], %[v1], v[252:255], %[oa]\n\t"
    "v_exp_f32_e32 v236, v236\n\t"
    "v_mov_b32_sdwa v249, v234 dst_sel:WORD_0 dst_unused:UNUSED_PRESERVE src0_sel:WORD_1\n\t"
    "v_exp_f32_e32 v251, v239\n\t"
    "v_exp_f32_e32 v238, v238\n\t"
    "v_mov_b32_sdwa v250, v236 dst_sel:WORD_0 dst_unused:UNUSED_PRESERVE src0_sel:WORD_1\n\t"
    "v_exp_f32_e32 v252, v241\n\t"
    "v_mov_b32_sdwa v251, v238 dst_sel:WORD_0 dst_unused:UNUSED_PRESERVE src0_sel:WORD_1\n\t"
    "v_exp_f32_e32 v240, v240\n\t"
    "v_exp_f32_e32 v253, v243\n\t"
    "v_mfma_f32_32x32x16_bf16 %[ob], %[v0], v[248:251], %[ob]\n\t"
    "ds_read_b128 %[v0], %[va] offset:3264\n\t"
    "v_exp_f32_e32 v242, v242\n\t"
    "v_mov_b32_sdwa v252, v240 dst_sel:WORD_0 dst_unused:UNUSED_PRESERVE src0_sel:WORD_1\n\t"
    "v_exp_f32_e32 v254, v245\n\t"
    "v_exp_f32_e32 v244, v244\n\t"
    "v_mov_b32_sdwa v253, v242 dst_sel:WORD_0 dst_unused:UNUSED_PRESERVE src0_sel:WORD_1\n\t"
    "v_exp_f32_e32 v255, v247\n\t"
    "v_exp_f32_e32 v246, v246\n\t"
    "v_mov_b32_sdwa v254, v244 dst_sel:WORD_0 dst_unused:UNUSED_PRESERVE src0_sel:WORD_1\n\t"
    "v_mov_b32_sdwa v255, v246 dst_sel:WORD_0 dst_unused:UNUSED_PRESERVE src0_sel:WORD_1\n\t"
    "v_mfma_f32_32x32x16_bf16 v[232:247], %[k], %[qb], %[nb]\n\t"
    "ds_read_b128 %[k], %[ka] offset:4096\n\t"
    "v_exp_f32_e32 v248, v217\n\t"
    "v_exp_f32_e32 v216, v216\n\t"
    "v_exp_f32_e32 v249, v219\n\t"
    "v_exp_f32_e32 v218, v218\n\t"
    "v_mov_b32_sdwa v248, v216 dst_sel:WORD_0 dst_unused:UNUSED_PRESERVE src0_sel:WORD_1\n\t"
    "v_exp_f32_e32 v250, v221\n\t"
    "v_mfma_f32_32x32x16_bf16 %[ob], %[v1], v[252:255], %[ob]\n\t"
    "ds_read_b128 %[v1], %[va] offset:3808\n\t"
    "v_exp_f32_e32 v220, v220\n\t"
    "v_mov_b32_sdwa v249, v218 dst_sel:WORD_0 dst_unused:UNUSED_PRESERVE src0_sel:WORD_1\n\t"
    "v_exp_f32_e32 v251, v223\n\t"
    "v_exp_f32_e32 v222, v222\n\t"
    "v_mov_b32_sdwa v250, v220 dst_sel:WORD_0 dst_unused:UNUSED_PRESERVE src0_sel:WORD_1\n\t"
    "v_exp_f32_e32 v252, v225\n\t"
    "v_mov_b32_sdwa v251, v222 dst_sel:WORD_0 dst_unused:UNUSED_PRESERVE src0_sel:WORD_1\n\t"
    "v_exp_f32_e32 v224, v224\n\t"
    "v_exp_f32_e32 v253, v227\n\t"
    "s_waitcnt lgkmcnt(2)\n\t"
    "v_mfma_f32_32x32x16_bf16 %[oa], %[v0], v[248:251], %[oa]\n\t"
    "v_exp_f32_e32 v226, v226\n\t"
    "v_mov_b32_sdwa v252, v224 dst_sel:WORD_0 dst_unused:UNUSED_PRESERVE src0_sel:WORD_1\n\t"
    "v_exp_f32_e32 v254, v229\n\t"
    "v_exp_f32_e32 v228, v228\n\t"
    "v_mov_b32_sdwa v253, v226 dst_sel:WORD_0 dst_unused:UNUSED_PRESERVE src0_sel:WORD_1\n\t"
    "v_exp_f32_e32 v255, v231\n\t"
    "v_exp_f32_e32 v230, v230\n\t"
    "v_mov_b32_sdwa v254, v228 dst_sel:WORD_0 dst_unused:UNUSED_PRESERVE src0_sel:WORD_1\n\t"
    "v_mov_b32_sdwa v255, v230 dst_sel:WORD_0 dst_unused:UNUSED_PRESERVE src0_sel:WORD_1\n\t"
    "s_waitcnt lgkmcnt(1)\n\t"
    "v_mfma_f32_32x32x16_bf16 v[216:231], %[k], %[qa], %[na]\n\t"
    "v_exp_f32_e32 v248, v233\n\t"
    "v_exp_f32_e32 v232, v232\n\t"
    "v_exp_f32_e32 v249, v235\n\t"
    "v_exp_f32_e32 v234, v234\n\t"
    "v_mov_b32_sdwa v248, v232 dst_sel:WORD_0 dst_unused:UNUSED_PRESERVE src0_sel:WORD_1\n\t"
    "v_exp_f32_e32 v250, v237\n\t"
    "s_waitcnt lgkmcnt(0)\n\t"
    "v_mfma_f32_32x32x16_bf16 %[oa], %[v1], v[252:255], %[oa]\n\t"
    "v_exp_f32_e32 v236, v236\n\t"
    "v_mov_b32_sdwa v249, v234 dst_sel:WORD_0 dst_unused:UNUSED_PRESERVE src0_sel:WORD_1\n\t"
    "v_exp_f32_e32 v251, v239\n\t"
    "v_exp_f32_e32 v238, v238\n\t"
    "v_mov_b32_sdwa v250, v236 dst_sel:WORD_0 dst_unused:UNUSED_PRESERVE src0_sel:WORD_1\n\t"
    "v_exp_f32_e32 v252, v241\n\t"
    "v_mov_b32_sdwa v251, v238 dst_sel:WORD_0 dst_unused:UNUSED_PRESERVE src0_sel:WORD_1\n\t"
    "v_exp_f32_e32 v240, v240\n\t"
    "v_exp_f32_e32 v253, v243\n\t"
    "v_mfma_f32_32x32x16_bf16 %[ob], %[v0], v[248:251], %[ob]\n\t"
    "ds_read_b128 %[v0], %[va] offset:4352\n\t"
    "v_exp_f32_e32 v242, v242\n\t"
    "v_mov_b32_sdwa v252, v240 dst_sel:WORD_0 dst_unused:UNUSED_PRESERVE src0_sel:WORD_1\n\t"
    "v_exp_f32_e32 v254, v245\n\t"
    "v_exp_f32_e32 v244, v244\n\t"
    "v_mov_b32_sdwa v253, v242 dst_sel:WORD_0 dst_unused:UNUSED_PRESERVE src0_sel:WORD_1\n\t"
    "v_exp_f32_e32 v255, v247\n\t"
    "v_exp_f32_e32 v246, v246\n\t"
    "s_add_u32 s91, s91, 1\n\t"
    "v_mov_b32_sdwa v254, v244 dst_sel:WORD_0 dst_unused:UNUSED_PRESERVE src0_sel:WORD_1\n\t"
    "v_add_u32_e32 %[ka], 4096, %[ka]\n\t"
    "v_mov_b32_sdwa v255, v246 dst_sel:WORD_0 dst_unused:UNUSED_PRESERVE src0_sel:WORD_1\n\t"
    "v_add_u32_e32 %[va], 4352, %[va]\n\t"
    "s_cmp_eq_u32 s91, %[ib]\n\t"
    "s_cbranch_scc1 5f\n\t"
    "6:\n\t"
    "s_cmp_lg_u32 s91, %[pd]\n\t"
    "s_cbranch_scc1 7f\n\t"
    "s_setprio 0\n\t"
    "7:\n\t"
    "s_cmp_lt_u32 s91, %[ns]\n\t"
    "s_cbranch_scc1 1b\n\t"
    "s_waitcnt lgkmcnt(0)\n\t"
    "v_mfma_f32_32x32x16_bf16 %[ob], %[v1], v[252:255], %[ob]\n\t"
    "s_branch 9f\n\t"
    "5:\n\t"
    "buffer_load_dwordx4 v[128:131], %[wv], %[rsw], %[wso] offen offset:0\n\t"
    "buffer_load_dwordx4 v[132:135], %[wv], %[rsw], %[wso] offen offset:1024\n\t"
    "buffer_load_dwordx4 v[136:139], %[wv], %[rsw], %[wso] offen offset:2048\n\t"
    "buffer_load_dwordx4 v[140:143], %[wv], %[rsw], %[wso] offen offset:3072\n\t"
    "s_add_u32 %[wso], %[wso], 4096\n\t"
    "buffer_load_dwordx4 v[144:147], %[wv], %[rsw], %[wso] offen offset:0\n\t"
    "buffer_load_dwordx4 v[148:151], %[wv], %[rsw], %[wso] offen offset:1024\n\t"
    "buffer_load_dwordx4 v[152:155], %[wv], %[rsw], %[wso] offen offset:2048\n\t"
    "buffer_load_dwordx4 v[156:159], %[wv], %[rsw], %[wso] offen offset:3072\n\t"
    "s_add_u32 %[wso], %[wso], 4096\n\t"
    "buffer_load_dwordx4 v[160:163], %[wv], %[rsw], %[wso] offen offset:0\n\t"
    "buffer_load_dwordx4 v[164:167], %[wv], %[rsw], %[wso] offen offset:1024\n\t"
    "buffer_load_dwordx4 v[168:171], %[wv], %[rsw], %[wso] offen offset:2048\n\t"
    "buffer_load_dwordx4 v[172:175], %[wv], %[rsw], %[wso] offen offset:3072\n\t"
    "s_add_u32 %[wso], %[wso], 4096\n\t"
    "buffer_load_dwordx4 v[176:179], %[wv], %[rsw], %[wso] offen offset:0\n\t"
    "buffer_load_dwordx4 v[180:183], %[wv], %[rsw], %[wso] offen offset:1024\n\t"
    "buffer_load_dwordx4 v[184:187], %[wv], %[rsw], %[wso] offen offset:2048\n\t"
    "buffer_load_dwordx4 v[188:191], %[wv], %[rsw], %[wso] offen offset:3072\n\t"
    "s_add_u32 %[wso], %[wso], 4096\n\t"
    "buffer_load_dwordx4 v[192:195], %[wv], %[rsx], %[xso] offen offset:0\n\t"
    "buffer_load_dwordx4 v[196:199], %[wv], %[rsx], %[xso] offen offset:1024\n\t"
    "buffer_load_dwordx4 v[200:203], %[wv], %[rsx], %[xso] offen offset:2048\n\t"
    "buffer_load_dwordx4 v[204:207], %[wv], %[rsx], %[xso] offen offset:3072\n\t"
    "s_branch 6b\n\t"
    "9:\n\t"
    "s_waitcnt vmcnt(0)\n\t"
    "s_nop 15\n\t"
        : [sc] "=&{v[216:247]}"(sc), [pp] "=&{v[248:255]}"(pp), [w0] "+{v[128:159]}"(w0), [w1] "+{v[160:191]}"(w1), [xb] "+{v[192:207]}"(xb),
          [oa] "+v"(oa), [ob] "+v"(ob), [k] "=&v"(kf), [v0] "+v"(v0), [v1] "+v"(v1), [ka] "+v"(ka), [va] "+v"(va), [wso] "+s"(wso)
        : [qa] "v"(qa), [qb] "v"(qb), [na] "v"(na), [nb] "v"(nb), [ib] "s"(ib), [ns] "s"(ns), [pd] "s"(pd), [wv] "v"(wv), [rsw] "s"(rsw), [rsx] "s"(rsx), [xso] "s"(xso)
        : "memory", "scc", "s91");
}
DI float halfmax(float m) {
    auto sw = __builtin_amdgcn_permlane32_swap(__float_as_uint(m), __float_as_uint(m), false, false);
    return fmaxf(__uint_as_float(sw[0]), __uint_as_float(sw[1]));
}
DI float max16(const f32x16& s) {
    float m = s[0];
#pragma unroll
    for (int i = 1; i < 16; ++i) m = fmaxf(m, s[i]);
    return m;
}
DI bool finite_acc(const f32x16& o) {
    bool ok = true;
#pragma unroll
    for (int i = 0; i < 9; ++i) ok = ok && (fabsf(o[i]) < 0x1p126f);
    return ok && (o[8] > 0.f);
}

struct AttnArgs { char* ws; u16* out; };

template <int M>
DI void out_proj(const u32x4 (&wf)[16], const char* xt, char* red, u16* out, int rt, int cg, int w, int l) {
    const int h = l >> 5, r = l & 31, c = w & 3, kh = w >> 2;
    f32x16 acc = {};
#pragma unroll
    for (int s = 0; s < 16; ++s) {
        const bf16x8 xf = *(const bf16x8*)(xt + (16 * kh + s) * 1024 + l * 16);
        acc = MFMA32(__builtin_bit_cast(bf16x8, wf[s]), xf, acc);
    }
    float* ex = (float*)red;
#pragma unroll
    for (int i = 0; i < 8; ++i) ex[(w * 8 + i) * 64 + l] = kh ? acc[i] : acc[8 + i];
    BAR();
    u32x4 ov;
#pragma unroll
    for (int j = 0; j < 4; ++j) {
        const float a0 = (kh ? acc[8 + 2 * j] : acc[2 * j]) + ex[((w ^ 4) * 8 + 2 * j) * 64 + l];
        const float a1 = (kh ? acc[8 + 2 * j + 1] : acc[2 * j + 1]) + ex[((w ^ 4) * 8 + 2 * j + 1) * 64 + l];
        ov[j] = pk_bf16(a0, a1);
    }
    *(u32x4*)(out + (size_t)(32 * rt + r) * 1024 + M * 512 + 128 * cg + 32 * c + 16 * kh + 8 * h) = ov;
}

DI void tail_phase(const AttnArgs& A, char* lds, const f32x16& oa, const f32x16& ob, float ma, float mb, const u32x4 (&wa)[16], const u32x4 (&xbr)[4]) {
    char* ws = A.ws;
    int tid = threadIdx.x; asm volatile("" : "+v"(tid));
    const int l = tid & 63, w = __builtin_amdgcn_readfirstlane(tid >> 6);
    const int b = blockIdx.x, rt = b >> 2, hg = b & 3;
    const int c = w & 3, z = w >> 2;
    const __amdgpu_buffer_rsrc_t rsW = __builtin_amdgcn_make_buffer_rsrc(ws + WS_WIMG, 0, 1u << 20, 0x00020000);
    const __amdgpu_buffer_rsrc_t rsO = __builtin_amdgcn_make_buffer_rsrc(ws + WS_OA, 0, S * DM * 2, 0x00020000);
    char* XA = lds; char* XBt = lds + SLOT; char* redA = lds + 2 * SLOT; char* redB = lds + 2 * SLOT + 16384;
    float* ex = (float*)(lds + 135168);
    const float linv_a = 1.0f / oa[8], linv_b = 1.0f / ob[8];
    const float La = ma + __builtin_amdgcn_logf(oa[8]), Lb = mb + __builtin_amdgcn_logf(ob[8]);
#pragma unroll
    for (int i = 0; i < 8; ++i) ex[(w * 9 + i) * 64 + l] = z ? oa[i] * linv_a : ob[i] * linv_b;
    ex[(w * 9 + 8) * 64 + l] = z ? La : Lb;
    BAR();
    {
        const float Lo = z ? Lb : La, li = z ? linv_b : linv_a, Lp = ex[((w ^ 4) * 9 + 8) * 64 + l];
        const float Lm = fmaxf(Lo, Lp), wo_ = __builtin_amdgcn_exp2f(Lo - Lm), wp = __builtin_amdgcn_exp2f(Lp - Lm), inv = 1.0f / (wo_ + wp);
        const float co = wo_ * inv * li, cp = wp * inv;
        u32x4 ov;
#pragma unroll
        for (int j = 0; j < 4; ++j) {
            const float e0 = (z ? ob[2 * j] : oa[2 * j]) * co + ex[((w ^ 4) * 9 + 2 * j) * 64 + l] * cp;
            const float e1 = (z ? ob[2 * j + 1] : oa[2 * j + 1]) * co + ex[((w ^ 4) * 9 + 2 * j + 1) * 64 + l] * cp;
            ov[j] = pk_bf16(e0, e1);
        }
        const int fr = 8 * hg + 2 * c + z;
        __builtin_amdgcn_raw_buffer_store_b128(ov, rsO, l * 16, (rt * 32 + fr) * 1024, 16);
        *(u32x4*)(XA + fr * 1024 + l * 16) = ov;
    }
#pragma unroll
    for (int i = 0; i < 4; ++i) *(u32x4*)(XBt + (4 * w + i) * 1024 + l * 16) = xbr[i];
    WAIT_VM(0);
    BAR();
    GLBP unsigned long long* flags = (GLBP unsigned long long*)(ws + WS_FLAG);
    if (tid == 0) __hip_atomic_store(flags + (size_t)b * 16, 1ull, __ATOMIC_RELAXED, __HIP_MEMORY_SCOPE_AGENT);
    STAMP(1, 3);
    u32x4 wb[16];
    const int wbo = (512 << 10) + ((4 * hg + c) * 32 + 16 * z) * 1024;
#pragma unroll
    for (int s = 0; s < 8; ++s) wb[s] = bload(rsW, l * 16, wbo + s * 1024);
    asm volatile("" ::: "memory");
    if (w == 0) {
        if (l < 4) { unsigned spins = 0; while (__hip_atomic_load(flags + (size_t)(4 * rt + l) * 16, __ATOMIC_RELAXED, __HIP_MEMORY_SCOPE_AGENT) != 1ull) { __builtin_amdgcn_s_sleep(1); if (++spins > (1u << 22)) break; } }
    }
    BAR();
    STAMP(1, 4);
    u32x4 t[3];
#pragma unroll
    for (int i = 0; i < 3; ++i) { const int fr = (8 * (hg + 1 + i) + w) & 31; t[i] = __builtin_bit_cast(u32x4, __builtin_amdgcn_raw_buffer_load_b128(rsO, l * 16, (rt * 32 + fr) * 1024, 16)); }
    asm volatile("" ::: "memory");
#pragma unroll
    for (int s = 8; s < 16; ++s) wb[s] = bload(rsW, l * 16, wbo + s * 1024);
    asm volatile("" ::: "memory");
#pragma unroll
    for (int i = 0; i < 3; ++i) *(u32x4*)(XA + ((8 * (hg + 1 + i) + w) & 31) * 1024 + l * 16) = t[i];
    BAR();
    STAMP(1, 5);
    out_proj<0>(wa, XA, redA, A.out, rt, hg, w, l);
    STAMP(1, 6);
    out_proj<1>(wb, XBt, redB, A.out, rt, hg, w, l);
    STAMP(1, 7);
}

DI void attn_phase(const AttnArgs& A, char* lds) {
    char* ws = A.ws;
    const int tid = threadIdx.x, l = tid & 63, w = __builtin_amdgcn_readfirstlane(tid >> 6);
    const int h = l >> 5, r = l & 31;
    const int b = blockIdx.x, rt = b >> 2, hg = b & 3;
    const int c = w & 3, z = w >> 2;
    const unsigned L0 = lds_addr(lds);
    STAMP(1, 0);
    const char* kvg = ws + WS_KV;
    const bf16x8 qa = *(const bf16x8*)(ws + WS_Q + (size_t)b * 8192 + (2 * c) * 1024 + l * 16);
    const bf16x8 qb = *(const bf16x8*)(ws + WS_Q + (size_t)b * 8192 + (2 * c + 1) * 1024 + l * 16);
#pragma unroll
    for (int i = 0; i < 17; ++i) { const int p = 8 * i + w; if (i < 16 || w < 4) glds16(kvg + p * 1024 + l * 16, L0 + p * 1024); }
    asm volatile("" :: "v"(qa), "v"(qb));
    WAIT_VM(0); BAR();
    STAMP(1, 1);
    const unsigned kaddr = L0 + z * 32768 + l * 16;
    const unsigned vaddr = L0 + 65536 + z * 64 * VGRP + (r < 16 ? h * 256 + r * 16 : ((r == 16 || r == 20) ? 512 : 528));
    float ma, mb;
    {
        const f32x16 zero = {};
        const bf16x8 k0 = *(const bf16x8*)(lds + z * 32768 + l * 16);
        ma = halfmax(max16(MFMA32(k0, qa, zero))) + MSHIFT; mb = halfmax(max16(MFMA32(k0, qb, zero))) + MSHIFT;
    }
    const __amdgpu_buffer_rsrc_t rsW = __builtin_amdgcn_make_buffer_rsrc(ws + WS_WIMG, 0, 1u << 20, 0x00020000);
    const __amdgpu_buffer_rsrc_t rsOB = __builtin_amdgcn_make_buffer_rsrc(ws + WS_OB, 0, S * DM * 2, 0x00020000);
    f32x32 w0 = {}, w1 = {}; f32x16 xb16 = {}, oa, ob;
    if (z) __builtin_amdgcn_s_setprio(1);
#pragma unroll 1
    for (int attempt = 0; attempt < 2; ++attempt) {
        att_pass(qa, qb, ma, mb, kaddr, vaddr, oa, ob, w0, w1, xb16, attempt ? 99u : (unsigned)(2 + c + z), 8u, PRIO_DROP,
                 rsW, rsOB, l * 16, ((4 * hg + c) * 32 + 16 * z) * 1024, (32 * rt + 4 * w) * 1024);
        STAMP(1, 2);
        if (attempt == 1 || __all(finite_acc(oa) && finite_acc(ob))) break;
        const f32x16 zero = {};
        float xa = -INFINITY, xb = -INFINITY;
#pragma unroll 1
        for (int t = 0; t < 32; ++t) {
            const bf16x8 kt = *(const bf16x8*)(lds + z * 32768 + l * 16 + t * 1024);
            xa = fmaxf(xa, max16(MFMA32(kt, qa, zero))); xb = fmaxf(xb, max16(MFMA32(kt, qb, zero)));
        }
        ma = halfmax(xa); mb = halfmax(xb);
    }
    u32x4 wa[16], xbr[4];
#pragma unroll
    for (int s = 0; s < 8; ++s)
#pragma unroll
        for (int j = 0; j < 4; ++j) { wa[s][j] = __float_as_uint(w0[4 * s + j]); wa[8 + s][j] = __float_as_uint(w1[4 * s + j]); }
#pragma unroll
    for (int i = 0; i < 4; ++i)
#pragma unroll
        for (int j = 0; j < 4; ++j) xbr[i][j] = __float_as_uint(xb16[4 * i + j]);
    tail_phase(A, lds, oa, ob, ma, mb, wa, xbr);
}


__global__ __launch_bounds__(512, 2) void k_front(FrontArgs a) {
    extern __shared__ __attribute__((aligned(16))) char lds[];
    front_phase(a, lds);
}
__global__ __launch_bounds__(512, 2) void k_attn(AttnArgs a) {
    extern __shared__ __attribute__((aligned(16))) char lds[];
    attn_phase(a, lds);
}
extern "C" void kernel_launch(void* const* d_in, const int* in_sizes, int n_in, void* d_out, int out_size, void* d_ws, size_t ws_size, hipStream_t stream) {
    FrontArgs fa;
    fa.xa = (const u16*)d_in[0]; fa.wq = (const u16*)d_in[1]; fa.wk = (const u16*)d_in[2]; fa.wv = (const u16*)d_in[3]; fa.woa = (const u16*)d_in[4];
    fa.xb = (const u16*)d_in[5]; fa.wqkv = (const u16*)d_in[6]; fa.wob = (const u16*)d_in[7]; fa.ws = (char*)d_ws;
    AttnArgs aa; aa.ws = (char*)d_ws; aa.out = (u16*)d_out;
    constexpr int LDSB = 163840;
    (void)hipFuncSetAttribute((const void*)k_front, hipFuncAttributeMaxDynamicSharedMemorySize, LDSB);
    (void)hipFuncSetAttribute((const void*)k_attn, hipFuncAttributeMaxDynamicSharedMemorySize, LDSB);
    k_front<<<256, 512, LDSB, stream>>>(fa);
    k_attn<<<256, 512, LDSB, stream>>>(aa);
}
```

```cpp
    "s_mov_b32 s91, 0\n\t"
    "ds_read_b128 %[k], %[ka]\n\t"
    "ds_read_b128 %[v0], %[va]\n\t"
    "v_mov_b32_e32 v252, 0\n\t"
    "v_mov_b32_e32 v253, 0\n\t"
    "v_mov_b32_e32 v254, 0\n\t"
    "v_mov_b32_e32 v255, 0\n\t"
    "s_waitcnt lgkmcnt(0)\n\t"
    "v_mfma_f32_32x32x16_bf16 v[216:231], %[k], %[qa], %[na]\n\t"
    "s_nop 11\n\t"
    "1:\n\t"
    "v_mfma_f32_32x32x16_bf16 v[232:247], %[k], %[qb], %[nb]\n\t"
    "ds_read_b128 %[k], %[ka] offset:1024\n\t"
    "v_exp_f32_e32 v248, v217\n\t"
    "v_exp_f32_e32 v216, v216\n\t"
    "v_exp_f32_e32 v249, v219\n\t"
    "v_exp_f32_e32 v218, v218\n\t"
    "v_mov_b32_sdwa v248, v216 dst_sel:WORD_0 dst_unused:UNUSED_PRESERVE src0_sel:WORD_1\n\t"
    "v_exp_f32_e32 v250, v221\n\t"
    "v_mfma_f32_32x32x16_bf16 %[ob], %[v1], v[252:255], %[ob]\n\t"
    "ds_read_b128 %[v1], %[va] offset:544\n\t"
    "v_exp_f32_e32 v220, v220\n\t"
    "v_mov_b32_sdwa v249, v218 dst_sel:WORD_0 dst_unused:UNUSED_PRESERVE src0_sel:WORD_1\n\t"
    "v_exp_f32_e32 v251, v223\n\t"
    "v_exp_f32_e32 v222, v222\n\t"
    "v_mov_b32_sdwa v250, v220 dst_sel:WORD_0 dst_unused:UNUSED_PRESERVE src0_sel:WORD_1\n\t"
    "v_exp_f32_e32 v252, v225\n\t"
    "v_mov_b32_sdwa v251, v222 dst_sel:WORD_0 dst_unused:UNUSED_PRESERVE src0_sel:WORD_1\n\t"
    "v_exp_f32_e32 v224, v224\n\t"
    "v_exp_f32_e32 v253, v227\n\t"
    "s_waitcnt lgkmcnt(2)\n\t"
    "v_mfma_f32_32x32x16_bf16 %[oa], %[v0], v[248:251], %[oa]\n\t"
    "v_exp_f32_e32 v226, v226\n\t"
    "v_mov_b32_sdwa v252, v224 dst_sel:WORD_0 dst_unused:UNUSED_PRESERVE src0_sel:WORD_1\n\t"
    "v_exp_f32_e32 v254, v229\n\t"
    "v_exp_f32_e32 v228, v228\n\t"
    "v_mov_b32_sdwa v253, v226 dst_sel:WORD_0 dst_unused:UNUSED_PRESERVE src0_sel:WORD_1\n\t"
    "v_exp_f32_e32 v255, v231\n\t"
    "v_exp_f32_e32 v230, v230\n\t"
    "v_mov_b32_sdwa v254, v228 dst_sel:WORD_0 dst_unused:UNUSED_PRESERVE src0_sel:WORD_1\n\t"
    "v_mov_b32_sdwa v255, v230 dst_sel:WORD_0 dst_unused:UNUSED_PRESERVE src0_sel:WORD_1\n\t"
    "s_waitcnt lgkmcnt(1)\n\t"
    "v_mfma_f32_32x32x16_bf16 v[216:231], %[k], %[qa], %[na]\n\t"
    "v_exp_f32_e32 v248, v233\n\t"
    "v_exp_f32_e32 v232, v232\n\t"
    "v_exp_f32_e32 v249, v235\n\t"
    "v_exp_f32_e32 v234, v234\n\t"
    "v_mov_b32_sdwa v248, v232 dst_sel:WORD_0 dst_unused:UNUSED_PRESERVE src0_sel:WORD_1\n\t"
    "v_exp_f32_e32 v250, v237\n\t"
    "s_waitcnt lgkmcnt(0)\n\t"
    "v_mfma_f32_32x32x16_bf16 %[oa], %[v1], v[252:255], %[oa]\n\t"
    "v_exp_f32_e32 v236, v236\n\t"
    "v_mov_b32_sdwa v249, v234 dst_sel:WORD_0 dst_unused:UNUSED_PRESERVE src0_sel:WORD_1\n\t"
    "v_exp_f32_e32 v251, v239\n\t"
    "v_exp_f32_e32 v238, v238\n\t"
    "v_mov_b32_sdwa v250, v236 dst_sel:WORD_0 dst_unused:UNUSED_PRESERVE src0_sel:WORD_1\n\t"
    "v_exp_f32_e32 v252, v241\n\t"
    "v_mov_b32_sdwa v251, v238 dst_sel:WORD_0 dst_unused:UNUSED_PRESERVE src0_sel:WORD_1\n\t"
    "v_exp_f32_e32 v240, v240\n\t"
    "v_exp_f32_e32 v253, v243\n\t"
    "v_mfma_f32_32x32x16_bf16 %[ob], %[v0], v[248:251], %[ob]\n\t"
    "ds_read_b128 %[v0], %[va] offset:1088\n\t"
    "v_exp_f32_e32 v242, v242\n\t"
    "v_mov_b32_sdwa v252, v240 dst_sel:WORD_0 dst_unused:UNUSED_PRESERVE src0_sel:WORD_1\n\t"
    "v_exp_f32_e32 v254, v245\n\t"
    "v_exp_f32_e32 v244, v244\n\t"
    "v_mov_b32_sdwa v253, v242 dst_sel:WORD_0 dst_unused:UNUSED_PRESERVE src0_sel:WORD_1\n\t"
    "v_exp_f32_e32 v255, v247\n\t"
    "v_exp_f32_e32 v246, v246\n\t"
    "v_mov_b32_sdwa v254, v244 dst_sel:WORD_0 dst_unused:UNUSED_PRESERVE src0_sel:WORD_1\n\t"
    "v_mov_b32_sdwa v255, v246 dst_sel:WORD_0 dst_unused:UNUSED_PRESERVE src0_sel:WORD_1\n\t"
    "v_mfma_f32_32x32x16_bf16 v[232:247], %[k], %[qb], %[nb]\n\t"
    "ds_read_b128 %[k], %[ka] offset:2048\n\t"
    "v_exp_f32_e32 v248, v217\n\t"
    "v_exp_f32_e32 v216, v216\n\t"
    "v_exp_f32_e32 v249, v219\n\t"
    "v_exp_f32_e32 v218, v218\n\t"
    "v_mov_b32_sdwa v248, v216 dst_sel:WORD_0 dst_unused:UNUSED_PRESERVE src0_sel:WORD_1\n\t"
    "v_exp_f32_e32 v250, v221\n\t"
    "v_mfma_f32_32x32x16_bf16 %[ob], %[v1], v[252:255], %[ob]\n\t"
    "ds_read_b128 %[v1], %[va] offset:1632\n\t"
    "v_exp_f32_e32 v220, v220\n\t"
    "v_mov_b32_sdwa v249, v218 dst_sel:WORD_0 dst_unused:UNUSED_PRESERVE src0_sel:WORD_1\n\t"
    "v_exp_f32_e32 v251, v223\n\t"
    "v_exp_f32_e32 v222, v222\n\t"
    "v_mov_b32_sdwa v250, v220 dst_sel:WORD_0 dst_unused:UNUSED_PRESERVE src0_sel:WORD_1\n\t"
    "v_exp_f32_e32 v252, v225\n\t"
    "v_mov_b32_sdwa v251, v222 dst_sel:WORD_0 dst_unused:UNUSED_PRESERVE src0_sel:WORD_1\n\t"
    "v_exp_f32_e32 v224, v224\n\t"
    "v_exp_f32_e32 v253, v227\n\t"
    "s_waitcnt lgkmcnt(2)\n\t"
    "v_mfma_f32_32x32x16_bf16 %[oa], %[v0], v[248:251], %[oa]\n\t"
    "v_exp_f32_e32 v226, v226\n\t"
    "v_mov_b32_sdwa v252, v224 dst_sel:WORD_0 dst_unused:UNUSED_PRESERVE src0_sel:WORD_1\n\t"
    "v_exp_f32_e32 v254, v229\n\t"
    "v_exp_f32_e32 v228, v228\n\t"
    "v_mov_b32_sdwa v253, v226 dst_sel:WORD_0 dst_unused:UNUSED_PRESERVE src0_sel:WORD_1\n\t"
    "v_exp_f32_e32 v255, v231\n\t"
    "v_exp_f32_e32 v230, v230\n\t"
    "v_mov_b32_sdwa v254, v228 dst_sel:WORD_0 dst_unused:UNUSED_PRESERVE src0_sel:WORD_1\n\t"
    "v_mov_b32_sdwa v255, v230 dst_sel:WORD_0 dst_unused:UNUSED_PRESERVE src0_sel:WORD_1\n\t"
    "s_waitcnt lgkmcnt(1)\n\t"
    "v_mfma_f32_32x32x16_bf16 v[216:231], %[k], %[qa], %[na]\n\t"
    "v_exp_f32_e32 v248, v233\n\t"
    "v_exp_f32_e32 v232, v232\n\t"
    "v_exp_f32_e32 v249, v235\n\t"
    "v_exp_f32_e32 v234, v234\n\t"
    "v_mov_b32_sdwa v248, v232 dst_sel:WORD_0 dst_unused:UNUSED_PRESERVE src0_sel:WORD_1\n\t"
    "v_exp_f32_e32 v250, v237\n\t"
    "s_waitcnt lgkmcnt(0)\n\t"
    "v_mfma_f32_32x32x16_bf16 %[oa], %[v1], v[252:255], %[oa]\n\t"
    "v_exp_f32_e32 v236, v236\n\t"
    "v_mov_b32_sdwa v249, v234 dst_sel:WORD_0 dst_unused:UNUSED_PRESERVE src0_sel:WORD_1\n\t"
    "v_exp_f32_e32 v251, v239\n\t"
    "v_exp_f32_e32 v238, v238\n\t"
    "v_mov_b32_sdwa v250, v236 dst_sel:WORD_0 dst_unused:UNUSED_PRESERVE src0_sel:WORD_1\n\t"
    "v_exp_f32_e32 v252, v241\n\t"
    "v_mov_b32_sdwa v251, v238 dst_sel:WORD_0 dst_unused:UNUSED_PRESERVE src0_sel:WORD_1\n\t"
    "v_exp_f32_e32 v240, v240\n\t"
    "v_exp_f32_e32 v253, v243\n\t"
    "v_mfma_f32_32x32x16_bf16 %[ob], %[v0], v[248:251], %[ob]\n\t"
    "ds_read_b128 %[v0], %[va] offset:2176\n\t"
    "v_exp_f32_e32 v242, v242\n\t"
    "v_mov_b32_sdwa v252, v240 dst_sel:WORD_0 dst_unused:UNUSED_PRESERVE src0_sel:WORD_1\n\t"
    "v_exp_f32_e32 v254, v245\n\t"
    "v_exp_f32_e32 v244, v244\n\t"
    "v_mov_b32_sdwa v253, v242 dst_sel:WORD_0 dst_unused:UNUSED_PRESERVE src0_sel:WORD_1\n\t"
    "v_exp_f32_e32 v255, v247\n\t"
    "v_exp_f32_e32 v246, v246\n\t"
    "v_mov_b32_sdwa v254, v244 dst_sel:WORD_0 dst_unused:UNUSED_PRESERVE src0_sel:WORD_1\n\t"
    "v_mov_b32_sdwa v255, v246 dst_sel:WORD_0 dst_unused:UNUSED_PRESERVE src0_sel:WORD_1\n\t"
    "v_mfma_f32_32x32x16_bf16 v[232:247], %[k], %[qb], %[nb]\n\t"
    "ds_read_b128 %[k], %[ka] offset:3072\n\t"
    "v_exp_f32_e32 v248, v217\n\t"
    "v_exp_f32_e32 v216, v216\n\t"
    "v_exp_f32_e32 v249, v219\n\t"
    "v_exp_f32_e32 v218, v218\n\t"
    "v_mov_b32_sdwa v248, v216 dst_sel:WORD_0 dst_unused:UNUSED_PRESERVE src0_sel:WORD_1\n\t"
    "v_exp_f32_e32 v250, v221\n\t"
    "v_mfma_f32_32x32x16_bf16 %[ob], %[v1], v[252:255], %[ob]\n\t"
    "ds_read_b128 %[v1], %[va] offset:2720\n\t"
    "v_exp_f32_e32 v220, v220\n\t"
    "v_mov_b32_sdwa v249, v218 dst_sel:WORD_0 dst_unused:UNUSED_PRESERVE src0_sel:WORD_1\n\t"
    "v_exp_f32_e32 v251, v223\n\t"
    "v_exp_f32_e32 v222, v222\n\t"
    "v_mov_b32_sdwa v250, v220 dst_sel:WORD_0 dst_unused:UNUSED_PRESERVE src0_sel:WORD_1\n\t"
    "v_exp_f32_e32 v252, v225\n\t"
    "v_mov_b32_sdwa v251, v222 dst_sel:WORD_0 dst_unused:UNUSED_PRESERVE src0_sel:WORD_1\n\t"
    "v_exp_f32_e32 v224, v224\n\t"
    "v_exp_f32_e32 v253, v227\n\t"
    "s_waitcnt lgkmcnt(2)\n\t"
    "v_mfma_f32_32x32x16_bf16 %[oa], %[v0], v[248:251], %[oa]\n\t"
    "v_exp_f32_e32 v226, v226\n\t"
    "v_mov_b32_sdwa v252, v224 dst_sel:WORD_0 dst_unused:UNUSED_PRESERVE src0_sel:WORD_1\n\t"
    "v_exp_f32_e32 v254, v229\n\t"
    "v_exp_f32_e32 v228, v228\n\t"
    "v_mov_b32_sdwa v253, v226 dst_sel:WORD_0 dst_unused:UNUSED_PRESERVE src0_sel:WORD_1\n\t"
    "v_exp_f32_e32 v255, v231\n\t"
    "v_exp_f32_e32 v230, v230\n\t"
    "v_mov_b32_sdwa v254, v228 dst_sel:WORD_0 dst_unused:UNUSED_PRESERVE src0_sel:WORD_1\n\t"
    "v_mov_b32_sdwa v255, v230 dst_sel:WORD_0 dst_unused:UNUSED_PRESERVE src0_sel:WORD_1\n\t"
    "s_waitcnt lgkmcnt(1)\n\t"
    "v_mfma_f32_32x32x16_bf16 v[216:231], %[k], %[qa], %[na]\n\t"
    "v_exp_f32_e32 v248, v233\n\t"
    "v_exp_f32_e32 v232, v232\n\t"
    "v_exp_f32_e32 v249, v235\n\t"
    "v_exp_f32_e32 v234, v234\n\t"
    "v_mov_b32_sdwa v248, v232 dst_sel:WORD_0 dst_unused:UNUSED_PRESERVE src0_sel:WORD_1\n\t"
    "v_exp_f32_e32 v250, v237\n\t"
    "s_waitcnt lgkmcnt(0)\n\t"
    "v_mfma_f32_32x32x16_bf16 %[oa], %[v1], v[252:255], %[oa]\n\t"
    "v_exp_f32_e32 v236, v236\n\t"
    "v_mov_b32_sdwa v249, v234 dst_sel:WORD_0 dst_unused:UNUSED_PRESERVE src0_sel:WORD_1\n\t"
    "v_exp_f32_e32 v251, v239\n\t"
    "v_exp_f32_e32 v238, v238\n\t"
    "v_mov_b32_sdwa v250, v236 dst_sel:WORD_0 dst_unused:UNUSED_PRESERVE src0_sel:WORD_1\n\t"
    "v_exp_f32_e32 v252, v241\n\t"
    "v_mov_b32_sdwa v251, v238 dst_sel:WORD_0 dst_unused:UNUSED_PRESERVE src0_sel:WORD_1\n\t"
    "v_exp_f32_e32 v240, v240\n\t"
    "v_exp_f32_e32 v253, v243\n\t"
    "v_mfma_f32_32x32x16_bf16 %[ob], %[v0], v[248:251], %[ob]\n\t"
    "ds_read_b128 %[v0], %[va] offset:3264\n\t"
    "v_exp_f32_e32 v242, v242\n\t"
    "v_mov_b32_sdwa v252, v240 dst_sel:WORD_0 dst_unused:UNUSED_PRESERVE src0_sel:WORD_1\n\t"
    "v_exp_f32_e32 v254, v245\n\t"
    "v_exp_f32_e32 v244, v244\n\t"
    "v_mov_b32_sdwa v253, v242 dst_sel:WORD_0 dst_unused:UNUSED_PRESERVE src0_sel:WORD_1\n\t"
    "v_exp_f32_e32 v255, v247\n\t"
    "v_exp_f32_e32 v246, v246\n\t"
    "v_mov_b32_sdwa v254, v244 dst_sel:WORD_0 dst_unused:UNUSED_PRESERVE src0_sel:WORD_1\n\t"
    "v_mov_b32_sdwa v255, v246 dst_sel:WORD_0 dst_unused:UNUSED_PRESERVE src0_sel:WORD_1\n\t"
    "v_mfma_f32_32x32x16_bf16 v[232:247], %[k], %[qb], %[nb]\n\t"
    "ds_read_b128 %[k], %[ka] offset:4096\n\t"
    "v_exp_f32_e32 v248, v217\n\t"
    "v_exp_f32_e32 v216, v216\n\t"
    "v_exp_f32_e32 v249, v219\n\t"
    "v_exp_f32_e32 v218, v218\n\t"
    "v_mov_b32_sdwa v248, v216 dst_sel:WORD_0 dst_unused:UNUSED_PRESERVE src0_sel:WORD_1\n\t"
    "v_exp_f32_e32 v250, v221\n\t"
    "v_mfma_f32_32x32x16_bf16 %[ob], %[v1], v[252:255], %[ob]\n\t"
    "ds_read_b128 %[v1], %[va] offset:3808\n\t"
    "v_exp_f32_e32 v220, v220\n\t"
    "v_mov_b32_sdwa v249, v218 dst_sel:WORD_0 dst_unused:UNUSED_PRESERVE src0_sel:WORD_1\n\t"
    "v_exp_f32_e32 v251, v223\n\t"
    "v_exp_f32_e32 v222, v222\n\t"
    "v_mov_b32_sdwa v250, v220 dst_sel:WORD_0 dst_unused:UNUSED_PRESERVE src0_sel:WORD_1\n\t"
    "v_exp_f32_e32 v252, v225\n\t"
    "v_mov_b32_sdwa v251, v222 dst_sel:WORD_0 dst_unused:UNUSED_PRESERVE src0_sel:WORD_1\n\t"
    "v_exp_f32_e32 v224, v224\n\t"
    "v_exp_f32_e32 v253, v227\n\t"
    "s_waitcnt lgkmcnt(2)\n\t"
    "v_mfma_f32_32x32x16_bf16 %[oa], %[v0], v[248:251], %[oa]\n\t"
    "v_exp_f32_e32 v226, v226\n\t"
    "v_mov_b32_sdwa v252, v224 dst_sel:WORD_0 dst_unused:UNUSED_PRESERVE src0_sel:WORD_1\n\t"
    "v_exp_f32_e32 v254, v229\n\t"
    "v_exp_f32_e32 v228, v228\n\t"
    "v_mov_b32_sdwa v253, v226 dst_sel:WORD_0 dst_unused:UNUSED_PRESERVE src0_sel:WORD_1\n\t"
    "v_exp_f32_e32 v255, v231\n\t"
    "v_exp_f32_e32 v230, v230\n\t"
    "v_mov_b32_sdwa v254, v228 dst_sel:WORD_0 dst_unused:UNUSED_PRESERVE src0_sel:WORD_1\n\t"
    "v_mov_b32_sdwa v255, v230 dst_sel:WORD_0 dst_unused:UNUSED_PRESERVE src0_sel:WORD_1\n\t"
    "s_waitcnt lgkmcnt(1)\n\t"
    "v_mfma_f32_32x32x16_bf16 v[216:231], %[k], %[qa], %[na]\n\t"
    "v_exp_f32_e32 v248, v233\n\t"
    "v_exp_f32_e32 v232, v232\n\t"
    "v_exp_f32_e32 v249, v235\n\t"
    "v_exp_f32_e32 v234, v234\n\t"
    "v_mov_b32_sdwa v248, v232 dst_sel:WORD_0 dst_unused:UNUSED_PRESERVE src0_sel:WORD_1\n\t"
    "v_exp_f32_e32 v250, v237\n\t"
    "s_waitcnt lgkmcnt(0)\n\t"
    "v_mfma_f32_32x32x16_bf16 %[oa], %[v1], v[252:255], %[oa]\n\t"
    "v_exp_f32_e32 v236, v236\n\t"
    "v_mov_b32_sdwa v249, v234 dst_sel:WORD_0 dst_unused:UNUSED_PRESERVE src0_sel:WORD_1\n\t"
    "v_exp_f32_e32 v251, v239\n\t"
    "v_exp_f32_e32 v238, v238\n\t"
    "v_mov_b32_sdwa v250, v236 dst_sel:WORD_0 dst_unused:UNUSED_PRESERVE src0_sel:WORD_1\n\t"
    "v_exp_f32_e32 v252, v241\n\t"
    "v_mov_b32_sdwa v251, v238 dst_sel:WORD_0 dst_unused:UNUSED_PRESERVE src0_sel:WORD_1\n\t"
    "v_exp_f32_e32 v240, v240\n\t"
    "v_exp_f32_e32 v253, v243\n\t"
    "v_mfma_f32_32x32x16_bf16 %[ob], %[v0], v[248:251], %[ob]\n\t"
    "ds_read_b128 %[v0], %[va] offset:4352\n\t"
    "v_exp_f32_e32 v242, v242\n\t"
    "v_mov_b32_sdwa v252, v240 dst_sel:WORD_0 dst_unused:UNUSED_PRESERVE src0_sel:WORD_1\n\t"
    "v_exp_f32_e32 v254, v245\n\t"
    "v_exp_f32_e32 v244, v244\n\t"
    "v_mov_b32_sdwa v253, v242 dst_sel:WORD_0 dst_unused:UNUSED_PRESERVE src0_sel:WORD_1\n\t"
    "v_exp_f32_e32 v255, v247\n\t"
    "v_exp_f32_e32 v246, v246\n\t"
    "s_add_u32 s91, s91, 1\n\t"
    "v_mov_b32_sdwa v254, v244 dst_sel:WORD_0 dst_unused:UNUSED_PRESERVE src0_sel:WORD_1\n\t"
    "v_add_u32_e32 %[ka], 4096, %[ka]\n\t"
    "v_mov_b32_sdwa v255, v246 dst_sel:WORD_0 dst_unused:UNUSED_PRESERVE src0_sel:WORD_1\n\t"
    "v_add_u32_e32 %[va], 4352, %[va]\n\t"
    "s_cmp_eq_u32 s91, %[ib]\n\t"
    "s_cbranch_scc1 5f\n\t"
    "6:\n\t"
    "s_cmp_lg_u32 s91, %[pd]\n\t"
    "s_cbranch_scc1 7f\n\t"
    "s_setprio 0\n\t"
    "7:\n\t"
    "s_cmp_lt_u32 s91, %[ns]\n\t"
    "s_cbranch_scc1 1b\n\t"
    "s_waitcnt lgkmcnt(0)\n\t"
    "v_mfma_f32_32x32x16_bf16 %[ob], %[v1], v[252:255], %[ob]\n\t"
    "s_branch 9f\n\t"
    "5:\n\t"
    "buffer_load_dwordx4 v[128:131], %[wv], %[rsw], %[wso] offen offset:0\n\t"
    "buffer_load_dwordx4 v[132:135], %[wv], %[rsw], %[wso] offen offset:1024\n\t"
    "buffer_load_dwordx4 v[136:139], %[wv], %[rsw], %[wso] offen offset:2048\n\t"
    "buffer_load_dwordx4 v[140:143], %[wv], %[rsw], %[wso] offen offset:3072\n\t"
    "s_add_u32 %[wso], %[wso], 4096\n\t"
    "buffer_load_dwordx4 v[144:147], %[wv], %[rsw], %[wso] offen offset:0\n\t"
    "buffer_load_dwordx4 v[148:151], %[wv], %[rsw], %[wso] offen offset:1024\n\t"
    "buffer_load_dwordx4 v[152:155], %[wv], %[rsw], %[wso] offen offset:2048\n\t"
    "buffer_load_dwordx4 v[156:159], %[wv], %[rsw], %[wso] offen offset:3072\n\t"
    "s_add_u32 %[wso], %[wso], 4096\n\t"
    "buffer_load_dwordx4 v[160:163], %[wv], %[rsw], %[wso] offen offset:0\n\t"
    "buffer_load_dwordx4 v[164:167], %[wv], %[rsw], %[wso] offen offset:1024\n\t"
    "buffer_load_dwordx4 v[168:171], %[wv], %[rsw], %[wso] offen offset:2048\n\t"
    "buffer_load_dwordx4 v[172:175], %[wv], %[rsw], %[wso] offen offset:3072\n\t"
    "s_add_u32 %[wso], %[wso], 4096\n\t"
    "buffer_load_dwordx4 v[176:179], %[wv], %[rsw], %[wso] offen offset:0\n\t"
    "buffer_load_dwordx4 v[180:183], %[wv], %[rsw], %[wso] offen offset:1024\n\t"
    "buffer_load_dwordx4 v[184:187], %[wv], %[rsw], %[wso] offen offset:2048\n\t"
    "buffer_load_dwordx4 v[188:191], %[wv], %[rsw], %[wso] offen offset:3072\n\t"
    "s_add_u32 %[wso], %[wso], 4096\n\t"
    "buffer_load_dwordx4 v[192:195], %[wv], %[rsx], %[xso] offen offset:0\n\t"
    "buffer_load_dwordx4 v[196:199], %[wv], %[rsx], %[xso] offen offset:1024\n\t"
    "buffer_load_dwordx4 v[200:203], %[wv], %[rsx], %[xso] offen offset:2048\n\t"
    "buffer_load_dwordx4 v[204:207], %[wv], %[rsx], %[xso] offen offset:3072\n\t"
    "s_branch 6b\n\t"
    "9:\n\t"
    "s_waitcnt vmcnt(0)\n\t"
    "s_nop 15\n\t"
        : [sc] "=&{v[216:247]}"(sc), [pp] "=&{v[248:255]}"(pp), [w0] "+{v[128:159]}"(w0), [w1] "+{v[160:191]}"(w1), [xb] "+{v[192:207]}"(xb),
          [oa] "+v"(oa), [ob] "+v"(ob), [k] "=&v"(kf), [v0] "+v"(v0), [v1] "+v"(v1), [ka] "+v"(ka), [va] "+v"(va), [wso] "+s"(wso)
        : [qa] "v"(qa), [qb] "v"(qb), [na] "v"(na), [nb] "v"(nb), [ib] "s"(ib), [ns] "s"(ns), [pd] "s"(pd), [wv] "v"(wv), [rsw] "s"(rsw), [rsx] "s"(rsx), [xso] "s"(xso)
        : "memory", "scc", "s91");
}
DI float halfmax(float m) {
    auto sw = __builtin_amdgcn_permlane32_swap(__float_as_uint(m), __float_as_uint(m), false, false);
    return fmaxf(__uint_as_float(sw[0]), __uint_as_float(sw[1]));
}
DI float max16(const f32x16& s) {
    float m = s[0];
#pragma unroll
    for (int i = 1; i < 16; ++i) m = fmaxf(m, s[i]);
    return m;
}
DI bool finite_acc(const f32x16& o) {
    bool ok = true;
#pragma unroll
    for (int i = 0; i < 9; ++i) ok = ok && (fabsf(o[i]) < 0x1p126f);
    return ok && (o[8] > 0.f);
}

struct AttnArgs { char* ws; u16* out; };

template <int M>
DI void out_proj(const u32x4 (&wf)[16], const char* xt, char* red, u16* out, int rt, int cg, int w, int l) {
    const int h = l >> 5, r = l & 31, c = w & 3, kh = w >> 2;
    f32x16 acc = {};
#pragma unroll
    for (int s = 0; s < 16; ++s) {
        const bf16x8 xf = *(const bf16x8*)(xt + (16 * kh + s) * 1024 + l * 16);
        acc = MFMA32(__builtin_bit_cast(bf16x8, wf[s]), xf, acc);
    }
    float* ex = (float*)red;
#pragma unroll
    for (int i = 0; i < 8; ++i) ex[(w * 8 + i) * 64 + l] = kh ? acc[i] : acc[8 + i];
    BAR();
    u32x4 ov;
#pragma unroll
    for (int j = 0; j < 4; ++j) {
        const float a0 = (kh ? acc[8 + 2 * j] : acc[2 * j]) + ex[((w ^ 4) * 8 + 2 * j) * 64 + l];
        const float a1 = (kh ? acc[8 + 2 * j + 1] : acc[2 * j + 1]) + ex[((w ^ 4) * 8 + 2 * j + 1) * 64 + l];
        ov[j] = pk_bf16(a0, a1);
    }
    *(u32x4*)(out + (size_t)(32 * rt + r) * 1024 + M * 512 + 128 * cg + 32 * c + 16 * kh + 8 * h) = ov;
}

DI void tail_phase(const AttnArgs& A, char* lds, const f32x16& oa, const f32x16& ob, float ma, float mb, const u32x4 (&wa)[16], const u32x4 (&xbr)[4] STAMP_ARG) {
    char* ws = A.ws;
    int tid = threadIdx.x; asm volatile("" : "+v"(tid));
    const int l = tid & 63, w = __builtin_amdgcn_readfirstlane(tid >> 6);
    const int b = blockIdx.x, rt = b >> 2, hg = b & 3;
    const int c = w & 3, z = w >> 2;
    const __amdgpu_buffer_rsrc_t rsW = __builtin_amdgcn_make_buffer_rsrc(ws + WS_WIMG, 0, 1u << 20, 0x00020000);
    const __amdgpu_buffer_rsrc_t rsO = __builtin_amdgcn_make_buffer_rsrc(ws + WS_OA, 0, S * DM * 2, 0x00020000);
    char* XA = lds; char* XBt = lds + SLOT; char* redA = lds + 2 * SLOT; char* redB = lds + 2 * SLOT + 16384;
    float* ex = (float*)(lds + 135168);
    const float linv_a = 1.0f / oa[8], linv_b = 1.0f / ob[8];
    const float La = ma + __builtin_amdgcn_logf(oa[8]), Lb = mb + __builtin_amdgcn_logf(ob[8]);
#pragma unroll
    for (int i = 0; i < 8; ++i) ex[(w * 9 + i) * 64 + l] = z ? oa[i] * linv_a : ob[i] * linv_b;
    ex[(w * 9 + 8) * 64 + l] = z ? La : Lb;
    STAMP(1, 3);
    BAR();
    STAMP(1, 4);
    {
        const float Lo = z ? Lb : La, li = z ? linv_b : linv_a, Lp = ex[((w ^ 4) * 9 + 8) * 64 + l];
        const float Lm = fmaxf(Lo, Lp), wo_ = __builtin_amdgcn_exp2f(Lo - Lm), wp = __builtin_amdgcn_exp2f(Lp - Lm), inv = 1.0f / (wo_ + wp);
        const float co = wo_ * inv * li, cp = wp * inv;
        u32x4 ov;
#pragma unroll
        for (int j = 0; j < 4; ++j) {
            const float e0 = (z ? ob[2 * j] : oa[2 * j]) * co + ex[((w ^ 4) * 9 + 2 * j) * 64 + l] * cp;
            const float e1 = (z ? ob[2 * j + 1] : oa[2 * j + 1]) * co + ex[((w ^ 4) * 9 + 2 * j + 1) * 64 + l] * cp;
            ov[j] = pk_bf16(e0, e1);
        }
        const int fr = 8 * hg + 2 * c + z;
        __builtin_amdgcn_raw_buffer_store_b128(ov, rsO, l * 16, (rt * 32 + fr) * 1024, 16);
        *(u32x4*)(XA + fr * 1024 + l * 16) = ov;
    }
#pragma unroll
    for (int i = 0; i < 4; ++i) *(u32x4*)(XBt + (4 * w + i) * 1024 + l * 16) = xbr[i];
    STAMP(1, 5);
    WAIT_VM(0);
    STAMP(1, 6);
    BAR();
    GLBP unsigned long long* flags = (GLBP unsigned long long*)(ws + WS_FLAG);
    if (tid == 0) __hip_atomic_store(flags + (size_t)b * 16, 1ull, __ATOMIC_RELAXED, __HIP_MEMORY_SCOPE_AGENT);
    STAMP(1, 7);
    u32x4 wb[16];
    const int wbo = (512 << 10) + ((4 * hg + c) * 32 + 16 * z) * 1024;
#pragma unroll
    for (int s = 0; s < 8; ++s) wb[s] = bload(rsW, l * 16, wbo + s * 1024);
    asm volatile("" ::: "memory");
    STAMP(1, 8);
    if (w == 0) {
        if (l < 4) { unsigned spins = 0; while (__hip_atomic_load(flags + (size_t)(4 * rt + l) * 16, __ATOMIC_RELAXED, __HIP_MEMORY_SCOPE_AGENT) != 1ull) { __builtin_amdgcn_s_sleep(1); if (++spins > (1u << 22)) break; } }
    }
    BAR();
    STAMP(1, 9);
    u32x4 t[3];
#pragma unroll
    for (int i = 0; i < 3; ++i) { const int fr = (8 * (hg + 1 + i) + w) & 31; t[i] = __builtin_bit_cast(u32x4, __builtin_amdgcn_raw_buffer_load_b128(rsO, l * 16, (rt * 32 + fr) * 1024, 16)); }
    asm volatile("" ::: "memory");
#pragma unroll
    for (int s = 8; s < 16; ++s) wb[s] = bload(rsW, l * 16, wbo + s * 1024);
    asm volatile("" ::: "memory");
    STAMP(1, 10);
#pragma unroll
    for (int i = 0; i < 3; ++i) *(u32x4*)(XA + ((8 * (hg + 1 + i) + w) & 31) * 1024 + l * 16) = t[i];
    STAMP(1, 11);
    BAR();
    STAMP(1, 12);
    out_proj<0>(wa, XA, redA, A.out, rt, hg, w, l);
    STAMP(1, 13);
    out_proj<1>(wb, XBt, redB, A.out, rt, hg, w, l);
    STAMP(1, 14);
    STAMP_FLUSH(1, 15);
}

DI void attn_phase(const AttnArgs& A, char* lds) {
    char* ws = A.ws;
    STAMP_DECL;
    const int tid = threadIdx.x, l = tid & 63, w = __builtin_amdgcn_readfirstlane(tid >> 6);
    const int h = l >> 5, r = l & 31;
    const int b = blockIdx.x, rt = b >> 2, hg = b & 3;
    const int c = w & 3, z = w >> 2;
    const unsigned L0 = lds_addr(lds);
    STAMP(1, 0);
    const char* kvg = ws + WS_KV;
    const bf16x8 qa = *(const bf16x8*)(ws + WS_Q + (size_t)b * 8192 + (2 * c) * 1024 + l * 16);
    const bf16x8 qb = *(const bf16x8*)(ws + WS_Q + (size_t)b * 8192 + (2 * c + 1) * 1024 + l * 16);
#pragma unroll
    for (int i = 0; i < 17; ++i) { const int p = 8 * i + w; if (i < 16 || w < 4) glds16(kvg + p * 1024 + l * 16, L0 + p * 1024); }
    asm volatile("" :: "v"(qa), "v"(qb));
    WAIT_VM(0); BAR();
    STAMP(1, 1);
    const unsigned kaddr = L0 + z * 32768 + l * 16;
    const unsigned vaddr = L0 + 65536 + z * 64 * VGRP + (r < 16 ? h * 256 + r * 16 : ((r == 16 || r == 20) ? 512 : 528));
    float ma, mb;
    {
        const f32x16 zero = {};
        const bf16x8 k0 = *(const bf16x8*)(lds + z * 32768 + l * 16);
        ma = halfmax(max16(MFMA32(k0, qa, zero))) + MSHIFT; mb = halfmax(max16(MFMA32(k0, qb, zero))) + MSHIFT;
    }
    const __amdgpu_buffer_rsrc_t rsW = __builtin_amdgcn_make_buffer_rsrc(ws + WS_WIMG, 0, 1u << 20, 0x00020000);
    const __amdgpu_buffer_rsrc_t rsOB = __builtin_amdgcn_make_buffer_rsrc(ws + WS_OB, 0, S * DM * 2, 0x00020000);
    f32x32 w0 = {}, w1 = {}; f32x16 xb16 = {}, oa, ob;
    if (z) __builtin_amdgcn_s_setprio(1);
#pragma unroll 1
    for (int attempt = 0; attempt < 2; ++attempt) {
        att_pass(qa, qb, ma, mb, kaddr, vaddr, oa, ob, w0, w1, xb16, attempt ? 99u : (unsigned)(2 + c + z), 8u, PRIO_DROP,
                 rsW, rsOB, l * 16, ((4 * hg + c) * 32 + 16 * z) * 1024, (32 * rt + 4 * w) * 1024);
        STAMP(1, 2);
        if (attempt == 1 || __all(finite_acc(oa) && finite_acc(ob))) break;
        const f32x16 zero = {};
        float xa = -INFINITY, xb = -INFINITY;
#pragma unroll 1
        for (int t = 0; t < 32; ++t) {
            const bf16x8 kt = *(const bf16x8*)(lds + z * 32768 + l * 16 + t * 1024);
            xa = fmaxf(xa, max16(MFMA32(kt, qa, zero))); xb = fmaxf(xb, max16(MFMA32(kt, qb, zero)));
        }
        ma = halfmax(xa); mb = halfmax(xb);
    }
    u32x4 wa[16], xbr[4];
#pragma unroll
    for (int s = 0; s < 8; ++s)
#pragma unroll
        for (int j = 0; j < 4; ++j) { wa[s][j] = __float_as_uint(w0[4 * s + j]); wa[8 + s][j] = __float_as_uint(w1[4 * s + j]); }
#pragma unroll
    for (int i = 0; i < 4; ++i)
#pragma unroll
        for (int j = 0; j < 4; ++j) xbr[i][j] = __float_as_uint(xb16[4 * i + j]);
    tail_phase(A, lds, oa, ob, ma, mb, wa, xbr STAMP_PASS);
}


__global__ __launch_bounds__(512, 2) void k_front(FrontArgs a) {
    extern __shared__ __attribute__((aligned(16))) char lds[];
    front_phase(a, lds);
}
__global__ __launch_bounds__(512, 2) void k_attn(AttnArgs a) {
    extern __shared__ __attribute__((aligned(16))) char lds[];
    attn_phase(a, lds);
}
extern "C" void kernel_launch(void* const* d_in, const int* in_sizes, int n_in, void* d_out, int out_size, void* d_ws, size_t ws_size, hipStream_t stream) {
    FrontArgs fa;
    fa.xa = (const u16*)d_in[0]; fa.wq = (const u16*)d_in[1]; fa.wk = (const u16*)d_in[2]; fa.wv = (const u16*)d_in[3]; fa.woa = (const u16*)d_in[4];
    fa.xb = (const u16*)d_in[5]; fa.wqkv = (const u16*)d_in[6]; fa.wob = (const u16*)d_in[7]; fa.ws = (char*)d_ws;
    AttnArgs aa; aa.ws = (char*)d_ws; aa.out = (u16*)d_out;
    constexpr int LDSB = 163840;
    (void)hipFuncSetAttribute((const void*)k_front, hipFuncAttributeMaxDynamicSharedMemorySize, LDSB);
    (void)hipFuncSetAttribute((const void*)k_attn, hipFuncAttributeMaxDynamicSharedMemorySize, LDSB);
    k_front<<<256, 512, LDSB, stream>>>(fa);
    k_attn<<<256, 512, LDSB, stream>>>(aa);
}
```

```cpp
    "s_mov_b32 s91, 0\n\t"
    "ds_read_b128 %[k], %[ka]\n\t"
    "ds_read_b128 %[v0], %[va]\n\t"
    "v_mov_b32_e32 v252, 0\n\t"
    "v_mov_b32_e32 v253, 0\n\t"
    "v_mov_b32_e32 v254, 0\n\t"
    "v_mov_b32_e32 v255, 0\n\t"
    "s_waitcnt lgkmcnt(0)\n\t"
    "v_mfma_f32_32x32x16_bf16 v[216:231], %[k], %[qa], %[na]\n\t"
    "s_nop 11\n\t"
    "1:\n\t"
    "v_mfma_f32_32x32x16_bf16 v[232:247], %[k], %[qb], %[nb]\n\t"
    "ds_read_b128 %[k], %[ka] offset:1024\n\t"
    "v_exp_f32_e32 v248, v217\n\t"
    "v_exp_f32_e32 v216, v216\n\t"
    "v_exp_f32_e32 v249, v219\n\t"
    "v_exp_f32_e32 v218, v218\n\t"
    "v_mov_b32_sdwa v248, v216 dst_sel:WORD_0 dst_unused:UNUSED_PRESERVE src0_sel:WORD_1\n\t"
    "v_exp_f32_e32 v250, v221\n\t"
    "v_mfma_f32_32x32x16_bf16 %[ob], %[v1], v[252:255], %[ob]\n\t"
    "ds_read_b128 %[v1], %[va] offset:528\n\t"
    "v_exp_f32_e32 v220, v220\n\t"
    "v_mov_b32_sdwa v249, v218 dst_sel:WORD_0 dst_unused:UNUSED_PRESERVE src0_sel:WORD_1\n\t"
    "v_exp_f32_e32 v251, v223\n\t"
    "v_exp_f32_e32 v222, v222\n\t"
    "v_mov_b32_sdwa v250, v220 dst_sel:WORD_0 dst_unused:UNUSED_PRESERVE src0_sel:WORD_1\n\t"
    "v_exp_f32_e32 v252, v225\n\t"
    "v_mov_b32_sdwa v251, v222 dst_sel:WORD_0 dst_unused:UNUSED_PRESERVE src0_sel:WORD_1\n\t"
    "v_exp_f32_e32 v224, v224\n\t"
    "v_exp_f32_e32 v253, v227\n\t"
    "s_waitcnt lgkmcnt(2)\n\t"
    "v_mfma_f32_32x32x16_bf16 %[oa], %[v0], v[248:251], %[oa]\n\t"
    "v_exp_f32_e32 v226, v226\n\t"
    "v_mov_b32_sdwa v252, v224 dst_sel:WORD_0 dst_unused:UNUSED_PRESERVE src0_sel:WORD_1\n\t"
    "v_exp_f32_e32 v254, v229\n\t"
    "v_exp_f32_e32 v228, v228\n\t"
    "v_mov_b32_sdwa v253, v226 dst_sel:WORD_0 dst_unused:UNUSED_PRESERVE src0_sel:WORD_1\n\t"
    "v_exp_f32_e32 v255, v231\n\t"
    "v_exp_f32_e32 v230, v230\n\t"
    "v_mov_b32_sdwa v254, v228 dst_sel:WORD_0 dst_unused:UNUSED_PRESERVE src0_sel:WORD_1\n\t"
    "v_mov_b32_sdwa v255, v230 dst_sel:WORD_0 dst_unused:UNUSED_PRESERVE src0_sel:WORD_1\n\t"
    "s_waitcnt lgkmcnt(1)\n\t"
    "v_mfma_f32_32x32x16_bf16 v[216:231], %[k], %[qa], %[na]\n\t"
    "v_exp_f32_e32 v248, v233\n\t"
    "v_exp_f32_e32 v232, v232\n\t"
    "v_exp_f32_e32 v249, v235\n\t"
    "v_exp_f32_e32 v234, v234\n\t"
    "v_mov_b32_sdwa v248, v232 dst_sel:WORD_0 dst_unused:UNUSED_PRESERVE src0_sel:WORD_1\n\t"
    "v_exp_f32_e32 v250, v237\n\t"
    "s_waitcnt lgkmcnt(0)\n\t"
    "v_mfma_f32_32x32x16_bf16 %[oa], %[v1], v[252:255], %[oa]\n\t"
    "v_exp_f32_e32 v236, v236\n\t"
    "v_mov_b32_sdwa v249, v234 dst_sel:WORD_0 dst_unused:UNUSED_PRESERVE src0_sel:WORD_1\n\t"
    "v_exp_f32_e32 v251, v239\n\t"
    "v_exp_f32_e32 v238, v238\n\t"
    "v_mov_b32_sdwa v250, v236 dst_sel:WORD_0 dst_unused:UNUSED_PRESERVE src0_sel:WORD_1\n\t"
    "v_exp_f32_e32 v252, v241\n\t"
    "v_mov_b32_sdwa v251, v238 dst_sel:WORD_0 dst_unused:UNUSED_PRESERVE src0_sel:WORD_1\n\t"
    "v_exp_f32_e32 v240, v240\n\t"
    "v_exp_f32_e32 v253, v243\n\t"
    "v_mfma_f32_32x32x16_bf16 %[ob], %[v0], v[248:251], %[ob]\n\t"
    "ds_read_b128 %[v0], %[va] offset:1056\n\t"
    "v_exp_f32_e32 v242, v242\n\t"
    "v_mov_b32_sdwa v252, v240 dst_sel:WORD_0 dst_unused:UNUSED_PRESERVE src0_sel:WORD_1\n\t"
    "v_exp_f32_e32 v254, v245\n\t"
    "v_exp_f32_e32 v244, v244\n\t"
    "v_mov_b32_sdwa v253, v242 dst_sel:WORD_0 dst_unused:UNUSED_PRESERVE src0_sel:WORD_1\n\t"
    "v_exp_f32_e32 v255, v247\n\t"
    "v_exp_f32_e32 v246, v246\n\t"
    "v_mov_b32_sdwa v254, v244 dst_sel:WORD_0 dst_unused:UNUSED_PRESERVE src0_sel:WORD_1\n\t"
    "v_mov_b32_sdwa v255, v246 dst_sel:WORD_0 dst_unused:UNUSED_PRESERVE src0_sel:WORD_1\n\t"
    "v_mfma_f32_32x32x16_bf16 v[232:247], %[k], %[qb], %[nb]\n\t"
    "ds_read_b128 %[k], %[ka] offset:2048\n\t"
    "v_exp_f32_e32 v248, v217\n\t"
    "v_exp_f32_e32 v216, v216\n\t"
    "v_exp_f32_e32 v249, v219\n\t"
    "v_exp_f32_e32 v218, v218\n\t"
    "v_mov_b32_sdwa v248, v216 dst_sel:WORD_0 dst_unused:UNUSED_PRESERVE src0_sel:WORD_1\n\t"
    "v_exp_f32_e32 v250, v221\n\t"
    "v_mfma_f32_32x32x16_bf16 %[ob], %[v1], v[252:255], %[ob]\n\t"
    "ds_read_b128 %[v1], %[va] offset:1584\n\t"
    "v_exp_f32_e32 v220, v220\n\t"
    "v_mov_b32_sdwa v249, v218 dst_sel:WORD_0 dst_unused:UNUSED_PRESERVE src0_sel:WORD_1\n\t"
    "v_exp_f32_e32 v251, v223\n\t"
    "v_exp_f32_e32 v222, v222\n\t"
    "v_mov_b32_sdwa v250, v220 dst_sel:WORD_0 dst_unused:UNUSED_PRESERVE src0_sel:WORD_1\n\t"
    "v_exp_f32_e32 v252, v225\n\t"
    "v_mov_b32_sdwa v251, v222 dst_sel:WORD_0 dst_unused:UNUSED_PRESERVE src0_sel:WORD_1\n\t"
    "v_exp_f32_e32 v224, v224\n\t"
    "v_exp_f32_e32 v253, v227\n\t"
    "s_waitcnt lgkmcnt(2)\n\t"
    "v_mfma_f32_32x32x16_bf16 %[oa], %[v0], v[248:251], %[oa]\n\t"
    "v_exp_f32_e32 v226, v226\n\t"
    "v_mov_b32_sdwa v252, v224 dst_sel:WORD_0 dst_unused:UNUSED_PRESERVE src0_sel:WORD_1\n\t"
    "v_exp_f32_e32 v254, v229\n\t"
    "v_exp_f32_e32 v228, v228\n\t"
    "v_mov_b32_sdwa v253, v226 dst_sel:WORD_0 dst_unused:UNUSED_PRESERVE src0_sel:WORD_1\n\t"
    "v_exp_f32_e32 v255, v231\n\t"
    "v_exp_f32_e32 v230, v230\n\t"
    "v_mov_b32_sdwa v254, v228 dst_sel:WORD_0 dst_unused:UNUSED_PRESERVE src0_sel:WORD_1\n\t"
    "v_mov_b32_sdwa v255, v230 dst_sel:WORD_0 dst_unused:UNUSED_PRESERVE src0_sel:WORD_1\n\t"
    "s_waitcnt lgkmcnt(1)\n\t"
    "v_mfma_f32_32x32x16_bf16 v[216:231], %[k], %[qa], %[na]\n\t"
    "v_exp_f32_e32 v248, v233\n\t"
    "v_exp_f32_e32 v232, v232\n\t"
    "v_exp_f32_e32 v249, v235\n\t"
    "v_exp_f32_e32 v234, v234\n\t"
    "v_mov_b32_sdwa v248, v232 dst_sel:WORD_0 dst_unused:UNUSED_PRESERVE src0_sel:WORD_1\n\t"
    "v_exp_f32_e32 v250, v237\n\t"
    "s_waitcnt lgkmcnt(0)\n\t"
    "v_mfma_f32_32x32x16_bf16 %[oa], %[v1], v[252:255], %[oa]\n\t"
    "v_exp_f32_e32 v236, v236\n\t"
    "v_mov_b32_sdwa v249, v234 dst_sel:WORD_0 dst_unused:UNUSED_PRESERVE src0_sel:WORD_1\n\t"
    "v_exp_f32_e32 v251, v239\n\t"
    "v_exp_f32_e32 v238, v238\n\t"
    "v_mov_b32_sdwa v250, v236 dst_sel:WORD_0 dst_unused:UNUSED_PRESERVE src0_sel:WORD_1\n\t"
    "v_exp_f32_e32 v252, v241\n\t"
    "v_mov_b32_sdwa v251, v238 dst_sel:WORD_0 dst_unused:UNUSED_PRESERVE src0_sel:WORD_1\n\t"
    "v_exp_f32_e32 v240, v240\n\t"
    "v_exp_f32_e32 v253, v243\n\t"
    "v_mfma_f32_32x32x16_bf16 %[ob], %[v0], v[248:251], %[ob]\n\t"
    "ds_read_b128 %[v0], %[va] offset:2112\n\t"
    "v_exp_f32_e32 v242, v242\n\t"
    "v_mov_b32_sdwa v252, v240 dst_sel:WORD_0 dst_unused:UNUSED_PRESERVE src0_sel:WORD_1\n\t"
    "v_exp_f32_e32 v254, v245\n\t"
    "v_exp_f32_e32 v244, v244\n\t"
    "v_mov_b32_sdwa v253, v242 dst_sel:WORD_0 dst_unused:UNUSED_PRESERVE src0_sel:WORD_1\n\t"
    "v_exp_f32_e32 v255, v247\n\t"
    "v_exp_f32_e32 v246, v246\n\t"
    "v_mov_b32_sdwa v254, v244 dst_sel:WORD_0 dst_unused:UNUSED_PRESERVE src0_sel:WORD_1\n\t"
    "v_mov_b32_sdwa v255, v246 dst_sel:WORD_0 dst_unused:UNUSED_PRESERVE src0_sel:WORD_1\n\t"
    "v_mfma_f32_32x32x16_bf16 v[232:247], %[k], %[qb], %[nb]\n\t"
    "ds_read_b128 %[k], %[ka] offset:3072\n\t"
    "v_exp_f32_e32 v248, v217\n\t"
    "v_exp_f32_e32 v216, v216\n\t"
    "v_exp_f32_e32 v249, v219\n\t"
    "v_exp_f32_e32 v218, v218\n\t"
    "v_mov_b32_sdwa v248, v216 dst_sel:WORD_0 dst_unused:UNUSED_PRESERVE src0_sel:WORD_1\n\t"
    "v_exp_f32_e32 v250, v221\n\t"
    "v_mfma_f32_32x32x16_bf16 %[ob], %[v1], v[252:255], %[ob]\n\t"
    "ds_read_b128 %[v1], %[va] offset:2640\n\t"
    "v_exp_f32_e32 v220, v220\n\t"
    "v_mov_b32_sdwa v249, v218 dst_sel:WORD_0 dst_unused:UNUSED_PRESERVE src0_sel:WORD_1\n\t"
    "v_exp_f32_e32 v251, v223\n\t"
    "v_exp_f32_e32 v222, v222\n\t"
    "v_mov_b32_sdwa v250, v220 dst_sel:WORD_0 dst_unused:UNUSED_PRESERVE src0_sel:WORD_1\n\t"
    "v_exp_f32_e32 v252, v225\n\t"
    "v_mov_b32_sdwa v251, v222 dst_sel:WORD_0 dst_unused:UNUSED_PRESERVE src0_sel:WORD_1\n\t"
    "v_exp_f32_e32 v224, v224\n\t"
    "v_exp_f32_e32 v253, v227\n\t"
    "s_waitcnt lgkmcnt(2)\n\t"
    "v_mfma_f32_32x32x16_bf16 %[oa], %[v0], v[248:251], %[oa]\n\t"
    "v_exp_f32_e32 v226, v226\n\t"
    "v_mov_b32_sdwa v252, v224 dst_sel:WORD_0 dst_unused:UNUSED_PRESERVE src0_sel:WORD_1\n\t"
    "v_exp_f32_e32 v254, v229\n\t"
    "v_exp_f32_e32 v228, v228\n\t"
    "v_mov_b32_sdwa v253, v226 dst_sel:WORD_0 dst_unused:UNUSED_PRESERVE src0_sel:WORD_1\n\t"
    "v_exp_f32_e32 v255, v231\n\t"
    "v_exp_f32_e32 v230, v230\n\t"
    "v_mov_b32_sdwa v254, v228 dst_sel:WORD_0 dst_unused:UNUSED_PRESERVE src0_sel:WORD_1\n\t"
    "v_mov_b32_sdwa v255, v230 dst_sel:WORD_0 dst_unused:UNUSED_PRESERVE src0_sel:WORD_1\n\t"
    "s_waitcnt lgkmcnt(1)\n\t"
    "v_mfma_f32_32x32x16_bf16 v[216:231], %[k], %[qa], %[na]\n\t"
    "v_exp_f32_e32 v248, v233\n\t"
    "v_exp_f32_e32 v232, v232\n\t"
    "v_exp_f32_e32 v249, v235\n\t"
    "v_exp_f32_e32 v234, v234\n\t"
    "v_mov_b32_sdwa v248, v232 dst_sel:WORD_0 dst_unused:UNUSED_PRESERVE src0_sel:WORD_1\n\t"
    "v_exp_f32_e32 v250, v237\n\t"
    "s_waitcnt lgkmcnt(0)\n\t"
    "v_mfma_f32_32x32x16_bf16 %[oa], %[v1], v[252:255], %[oa]\n\t"
    "v_exp_f32_e32 v236, v236\n\t"
    "v_mov_b32_sdwa v249, v234 dst_sel:WORD_0 dst_unused:UNUSED_PRESERVE src0_sel:WORD_1\n\t"
    "v_exp_f32_e32 v251, v239\n\t"
    "v_exp_f32_e32 v238, v238\n\t"
    "v_mov_b32_sdwa v250, v236 dst_sel:WORD_0 dst_unused:UNUSED_PRESERVE src0_sel:WORD_1\n\t"
    "v_exp_f32_e32 v252, v241\n\t"
    "v_mov_b32_sdwa v251, v238 dst_sel:WORD_0 dst_unused:UNUSED_PRESERVE src0_sel:WORD_1\n\t"
    "v_exp_f32_e32 v240, v240\n\t"
    "v_exp_f32_e32 v253, v243\n\t"
    "v_mfma_f32_32x32x16_bf16 %[ob], %[v0], v[248:251], %[ob]\n\t"
    "ds_read_b128 %[v0], %[va] offset:3168\n\t"
    "v_exp_f32_e32 v242, v242\n\t"
    "v_mov_b32_sdwa v252, v240 dst_sel:WORD_0 dst_unused:UNUSED_PRESERVE src0_sel:WORD_1\n\t"
    "v_exp_f32_e32 v254, v245\n\t"
    "v_exp_f32_e32 v244, v244\n\t"
    "v_mov_b32_sdwa v253, v242 dst_sel:WORD_0 dst_unused:UNUSED_PRESERVE src0_sel:WORD_1\n\t"
    "v_exp_f32_e32 v255, v247\n\t"
    "v_exp_f32_e32 v246, v246\n\t"
    "v_mov_b32_sdwa v254, v244 dst_sel:WORD_0 dst_unused:UNUSED_PRESERVE src0_sel:WORD_1\n\t"
    "v_mov_b32_sdwa v255, v246 dst_sel:WORD_0 dst_unused:UNUSED_PRESERVE src0_sel:WORD_1\n\t"
    "v_mfma_f32_32x32x16_bf16 v[232:247], %[k], %[qb], %[nb]\n\t"
    "ds_read_b128 %[k], %[ka] offset:4096\n\t"
    "v_exp_f32_e32 v248, v217\n\t"
    "v_exp_f32_e32 v216, v216\n\t"
    "v_exp_f32_e32 v249, v219\n\t"
    "v_exp_f32_e32 v218, v218\n\t"
    "v_mov_b32_sdwa v248, v216 dst_sel:WORD_0 dst_unused:UNUSED_PRESERVE src0_sel:WORD_1\n\t"
    "v_exp_f32_e32 v250, v221\n\t"
    "v_mfma_f32_32x32x16_bf16 %[ob], %[v1], v[252:255], %[ob]\n\t"
    "ds_read_b128 %[v1], %[va] offset:3696\n\t"
    "v_exp_f32_e32 v220, v220\n\t"
    "v_mov_b32_sdwa v249, v218 dst_sel:WORD_0 dst_unused:UNUSED_PRESERVE src0_sel:WORD_1\n\t"
    "v_exp_f32_e32 v251, v223\n\t"
    "v_exp_f32_e32 v222, v222\n\t"
    "v_mov_b32_sdwa v250, v220 dst_sel:WORD_0 dst_unused:UNUSED_PRESERVE src0_sel:WORD_1\n\t"
    "v_exp_f32_e32 v252, v225\n\t"
    "v_mov_b32_sdwa v251, v222 dst_sel:WORD_0 dst_unused:UNUSED_PRESERVE src0_sel:WORD_1\n\t"
    "v_exp_f32_e32 v224, v224\n\t"
    "v_exp_f32_e32 v253, v227\n\t"
    "s_waitcnt lgkmcnt(2)\n\t"
    "v_mfma_f32_32x32x16_bf16 %[oa], %[v0], v[248:251], %[oa]\n\t"
    "v_exp_f32_e32 v226, v226\n\t"
    "v_mov_b32_sdwa v252, v224 dst_sel:WORD_0 dst_unused:UNUSED_PRESERVE src0_sel:WORD_1\n\t"
    "v_exp_f32_e32 v254, v229\n\t"
    "v_exp_f32_e32 v228, v228\n\t"
    "v_mov_b32_sdwa v253, v226 dst_sel:WORD_0 dst_unused:UNUSED_PRESERVE src0_sel:WORD_1\n\t"
    "v_exp_f32_e32 v255, v231\n\t"
    "v_exp_f32_e32 v230, v230\n\t"
    "v_mov_b32_sdwa v254, v228 dst_sel:WORD_0 dst_unused:UNUSED_PRESERVE src0_sel:WORD_1\n\t"
    "v_mov_b32_sdwa v255, v230 dst_sel:WORD_0 dst_unused:UNUSED_PRESERVE src0_sel:WORD_1\n\t"
    "s_waitcnt lgkmcnt(1)\n\t"
    "v_mfma_f32_32x32x16_bf16 v[216:231], %[k], %[qa], %[na]\n\t"
    "v_exp_f32_e32 v248, v233\n\t"
    "v_exp_f32_e32 v232, v232\n\t"
    "v_exp_f32_e32 v249, v235\n\t"
    "v_exp_f32_e32 v234, v234\n\t"
    "v_mov_b32_sdwa v248, v232 dst_sel:WORD_0 dst_unused:UNUSED_PRESERVE src0_sel:WORD_1\n\t"
    "v_exp_f32_e32 v250, v237\n\t"
    "s_waitcnt lgkmcnt(0)\n\t"
    "v_mfma_f32_32x32x16_bf16 %[oa], %[v1], v[252:255], %[oa]\n\t"
    "v_exp_f32_e32 v236, v236\n\t"
    "v_mov_b32_sdwa v249, v234 dst_sel:WORD_0 dst_unused:UNUSED_PRESERVE src0_sel:WORD_1\n\t"
    "v_exp_f32_e32 v251, v239\n\t"
    "v_exp_f32_e32 v238, v238\n\t"
    "v_mov_b32_sdwa v250, v236 dst_sel:WORD_0 dst_unused:UNUSED_PRESERVE src0_sel:WORD_1\n\t"
    "v_exp_f32_e32 v252, v241\n\t"
    "v_mov_b32_sdwa v251, v238 dst_sel:WORD_0 dst_unused:UNUSED_PRESERVE src0_sel:WORD_1\n\t"
    "v_exp_f32_e32 v240, v240\n\t"
    "v_exp_f32_e32 v253, v243\n\t"
    "v_mfma_f32_32x32x16_bf16 %[ob], %[v0], v[248:251], %[ob]\n\t"
    "ds_read_b128 %[v0], %[va] offset:4224\n\t"
    "v_exp_f32_e32 v242, v242\n\t"
    "v_mov_b32_sdwa v252, v240 dst_sel:WORD_0 dst_unused:UNUSED_PRESERVE src0_sel:WORD_1\n\t"
    "v_exp_f32_e32 v254, v245\n\t"
    "v_exp_f32_e32 v244, v244\n\t"
    "v_mov_b32_sdwa v253, v242 dst_sel:WORD_0 dst_unused:UNUSED_PRESERVE src0_sel:WORD_1\n\t"
    "v_exp_f32_e32 v255, v247\n\t"
    "v_exp_f32_e32 v246, v246\n\t"
    "s_add_u32 s91, s91, 1\n\t"
    "v_mov_b32_sdwa v254, v244 dst_sel:WORD_0 dst_unused:UNUSED_PRESERVE src0_sel:WORD_1\n\t"
    "v_add_u32_e32 %[ka], 4096, %[ka]\n\t"
    "v_mov_b32_sdwa v255, v246 dst_sel:WORD_0 dst_unused:UNUSED_PRESERVE src0_sel:WORD_1\n\t"
    "v_add_u32_e32 %[va], 4224, %[va]\n\t"
    "s_cmp_lt_u32 s91, %[ns]\n\t"
    "s_cbranch_scc1 1b\n\t"
    "s_waitcnt lgkmcnt(0)\n\t"
    "v_mfma_f32_32x32x16_bf16 %[ob], %[v1], v[252:255], %[ob]\n\t"
    "s_nop 15\n\t"
        : [sc] "=&{v[216:247]}"(sc), [pp] "=&{v[248:255]}"(pp),
          [oa] "+v"(oa), [ob] "+v"(ob), [k] "=&v"(kf), [v0] "+v"(v0), [v1] "+v"(v1), [ka] "+v"(ka), [va] "+v"(va)
        : [qa] "v"(qa), [qb] "v"(qb), [na] "v"(na), [nb] "v"(nb), [ns] "s"(ns)
        : "memory", "scc", "s91");
}
DI float halfmax(float m) {
    auto sw = __builtin_amdgcn_permlane32_swap(__float_as_uint(m), __float_as_uint(m), false, false);
    return fmaxf(__uint_as_float(sw[0]), __uint_as_float(sw[1]));
}
DI float max16(const f32x16& s) {
    float m = s[0];
#pragma unroll
    for (int i = 1; i < 16; ++i) m = fmaxf(m, s[i]);
    return m;
}
DI float nonfinite_probe(const f32x16& o, float t) {
#pragma unroll
    for (int i = 0; i < 9; ++i) t = __builtin_fmaf(o[i], 0.f, t);
    return t;
}

DI void attention_wave(const Args& A, char* lds, u64 tag, const bf16x8 qa, const bf16x8 qb STAMP_ARG) {
    char* ws = A.ws;
    const int tid = threadIdx.x, l = tid & 63, c = __builtin_amdgcn_readfirstlane(tid >> 6);
    const int h = l >> 5, r = l & 31;
    const Ids id = ids_of(blockIdx.x);
    const unsigned L0 = lds_addr(lds);
    const unsigned kaddr = L0 + l * 16;
    const unsigned vaddr = r < 16 ? L0 + 65536 + h * 256 + r * 16 : ((r == 16 || r == 20) ? L0 + 65536 + 512 : 0x100000u);
    float ma, mb;
    {
        const f32x16 zero = {};
        const bf16x8 k0 = *(const bf16x8*)(lds + l * 16);
        ma = halfmax(max16(MFMA32(k0, qa, zero))) + MSHIFT; mb = halfmax(max16(MFMA32(k0, qb, zero))) + MSHIFT;
    }
    f32x16 oa, ob;
    STAMP(6);
#pragma unroll 1
    for (int attempt = 0; attempt < 2; ++attempt) {
        att_pass(qa, qb, ma, mb, kaddr, vaddr, oa, ob);
        STAMP(7);
        const float probe = nonfinite_probe(ob, nonfinite_probe(oa, 0.f));
        if (attempt == 1 || __all(probe == 0.f)) break;
        const f32x16 zero = {};
        float xa = -INFINITY, xb = -INFINITY;
#pragma unroll 1
        for (int t = 0; t < 64; ++t) {
            const bf16x8 kt = *(const bf16x8*)(lds + l * 16 + t * 1024);
            xa = fmaxf(xa, max16(MFMA32(kt, qa, zero))); xb = fmaxf(xb, max16(MFMA32(kt, qb, zero)));
        }
        ma = halfmax(xa); mb = halfmax(xb);
    }
    const u32x4 ova = pack8(oa, 0, __builtin_amdgcn_rcpf(oa[8])), ovb = pack8(ob, 0, __builtin_amdgcn_rcpf(ob[8]));
    const __amdgpu_buffer_rsrc_t rsO = __builtin_amdgcn_make_buffer_rsrc(ws + WS_OA, 0, S * DM * 2, 0x00020000);
    const int fr = 8 * id.hg + 2 * c;
    bstore_sc1(ova, rsO, l * 16, (id.rt * 32 + fr) * 1024); bstore_sc1(ovb, rsO, l * 16, (id.rt * 32 + fr + 1) * 1024);
    WAIT_VM(0);
    if (l == 0) __hip_atomic_store((GLBP u64*)(ws + WS_FLAG) + 1536 + (id.rt * 4 + id.hg) * 4 + c, tag, RLX_AGENT);
    STAMP(8);
    BAR();
    *(u32x4*)(lds + fr * 1024 + l * 16) = ova; *(u32x4*)(lds + (fr + 1) * 1024 + l * 16) = ovb;
    BAR();
    STAMP(9);
}

template <typename G, typename F>
DI void stream_w(const u16* wo, int col0, char* ring, unsigned ringL, int l, G&& after_first_issue, F&& consume) {
    const int h = l >> 5, blk = (l >> 4) & 1, q4 = (l & 15) >> 2, p4 = l & 3, sg = ((p4 & 1) << 1) | (p4 >> 1);
    const unsigned voff = ((l >> 2) * 512 + col0 + (l & 3) * 8) * 2;
    const char* rd = ring + (8 * h + q4) * 64 + blk * 32 + sg * 8;
    auto issue = [&](int ch) {
        glds16s((const char*)wo + (size_t)ch * 32 * 1024, voff, ringL + (ch % 3) * 2048);
        glds16s((const char*)wo + (size_t)ch * 32 * 1024 + 16 * 1024, voff, ringL + (ch % 3) * 2048 + 1024);
    };
    issue(0); issue(1); issue(2);
    after_first_issue();
#pragma unroll
    for (int ch = 0; ch < 16; ++ch) {
        if (ch <= 13) { WAIT_VM(4); } else if (ch == 14) { WAIT_VM(2); } else { WAIT_VM(0); }
        const char* p = rd + (ch % 3) * 2048;
        const bf16x8 f0 = tr_frag(p, p + 256), f1 = tr_frag(p + 1024, p + 1024 + 256);
        asm volatile("s_waitcnt lgkmcnt(0)" : "+v"(const_cast<bf16x8&>(f0)), "+v"(const_cast<bf16x8&>(f1)) :: "memory");
        if (ch + 3 < 16) issue(ch + 3);
        consume(2 * ch, f0); consume(2 * ch + 1, f1);
    }
}
DI void store_out(const f32x16& acc, u16* out, int rt, int col, int l) {
    u16* dst = out + (size_t)(32 * rt + (l & 31)) * 1024 + col + 8 * (l >> 5);
    *(u32x4*)dst = pack8(acc, 0, 1.f); *(u32x4*)(dst + 16) = pack8(acc, 8, 1.f);
}

DI void branch_wave(const Args& A, char* lds, u64 tag STAMP_ARG) {
    char* ws = A.ws;
    const int tid = threadIdx.x, l = tid & 63, c = __builtin_amdgcn_readfirstlane(tid >> 6) - 4;
    const Ids id = ids_of(blockIdx.x);
    const int rt = id.rt, hg = id.hg, brt = id.brt, hq = id.hq;
    const unsigned L0 = lds_addr(lds);
    GLBP u64* flags = (GLBP u64*)(ws + WS_FLAG);
    const __amdgpu_buffer_rsrc_t rsB = __builtin_amdgcn_make_buffer_rsrc(ws + WS_OB, 0, S * DM * 2, 0x00020000);
    __builtin_amdgcn_s_setprio(BPRIO);
    {
        const char* qt = lds + QT;
        const int lr = 16 * c + (l >> 2), hd = l & 3, grow = 64 * brt + lr;
        auto ld = [&](int row, int chunk) { return *(const u32x4*)(qt + row * 384 + ((chunk ^ (row & 7)) * 16)); };
        const u32x4 q0 = ld(16 + lr, 2 * hd), q1 = ld(16 + lr, 2 * hd + 1);
        float lg[8];
#pragma unroll
        for (int wv = 0; wv < 8; ++wv) {
            const u32x4 k0 = ld(16 + lr - 2 * wv, 8 + 2 * hd), k1 = ld(16 + lr - 2 * wv, 8 + 2 * hd + 1);
            float s0 = 0.f, s1 = 0.f;
#pragma unroll
            for (int i = 0; i < 4; ++i) { s0 = dot2(q0[i], k0[i], s0); s1 = dot2(q1[i], k1[i], s1); }
            lg[wv] = (grow - 2 * wv < 0) ? -INFINITY : (s0 + s1) * QSCALE;
        }
        float m = lg[0];
#pragma unroll
        for (int wv = 1; wv < 8; ++wv) m = fmaxf(m, lg[wv]);
        float pw[8], ls = 0.f;
#pragma unroll
        for (int wv = 0; wv < 8; ++wv) { pw[wv] = __builtin_amdgcn_exp2f(lg[wv] - m); ls += pw[wv]; }
        const float inv = __builtin_amdgcn_rcpf(ls);
        float o[16];
#pragma unroll
        for (int i = 0; i < 16; ++i) o[i] = 0.f;
#pragma unroll
        for (int wp = 0; wp < 4; ++wp) {
            const unsigned pp = pk_bf16(pw[2 * wp] * inv, pw[2 * wp + 1] * inv);
            const int ra = 16 + lr - 4 * wp, rb = ra - 2;
            const u32x4 a0 = ld(ra, 16 + 2 * hd), a1 = ld(ra, 16 + 2 * hd + 1), c0 = ld(rb, 16 + 2 * hd), c1 = ld(rb, 16 + 2 * hd + 1);
#pragma unroll
            for (int i = 0; i < 4; ++i) {
                o[2 * i] = dot2(__builtin_amdgcn_perm(c0[i], a0[i], 0x05040100u), pp, o[2 * i]);
                o[2 * i + 1] = dot2(__builtin_amdgcn_perm(c0[i], a0[i], 0x07060302u), pp, o[2 * i + 1]);
                o[8 + 2 * i] = dot2(__builtin_amdgcn_perm(c1[i], a1[i], 0x05040100u), pp, o[8 + 2 * i]);
                o[8 + 2 * i + 1] = dot2(__builtin_amdgcn_perm(c1[i], a1[i], 0x07060302u), pp, o[8 + 2 * i + 1]);
            }
        }
        u32x4 r0, r1;
#pragma unroll
        for (int i = 0; i < 4; ++i) { r0[i] = pk_bf16(o[2 * i], o[2 * i + 1]); r1[i] = pk_bf16(o[8 + 2 * i], o[8 + 2 * i + 1]); }
        const int off = ((grow >> 5) * 32 + 4 * hq + hd) * 1024 + (grow & 31) * 16;
        bstore_sc1(r0, rsB, off, 0); bstore_sc1(r1, rsB, off + 512, 0);
    }
    WAIT_VM(0);
    if (l == 0) __hip_atomic_store(flags + 512 + (8 * brt + hq) * 4 + c, tag, RLX_AGENT);
    STAMP(10);
    poll_flags(l < 32 ? flags + 512 + 32 * (rt >> 1) + l : flags + 512 + (8 * brt + hq) * 4 + (l & 3), l < 36, tag);
    STAMP(11);
    char* ring = lds + QT + c * 7680; const unsigned ringL = L0 + QT + c * 7680;
    const int col0 = 128 * hg + 32 * c;
    {
        u32x4 xb[32];
        f32x16 acc = {};
        stream_w(A.wob, col0, ring, ringL, l, [&]() {
#pragma unroll
            for (int s = 0; s < 32; ++s) xb[s] = bload_sc1(rsB, l * 16, (rt * 32 + s) * 1024);
#pragma unroll
            for (int s = 0; s < 32; ++s) asm volatile("" : "+v"(xb[s]));
        }, [&](int ks, const bf16x8& f) { acc = MFMA32(f, __builtin_bit_cast(bf16x8, xb[ks]), acc); });
        store_out(acc, A.out, rt, 512 + col0, l);
    }
    STAMP(12);
    bf16x8 wa[32];
    stream_w(A.woa, col0, ring, ringL, l, [&]() {}, [&](int ks, const bf16x8& f) { wa[ks] = f; });
    STAMP(13);
    u32x4 t[6];
    {
        const int f = (8 * (hg + 1) + 6 * c + (l < 6 ? l : 0)) & 31;
        poll_flags(flags + 1536 + (rt * 4 + (f >> 3)) * 4 + ((f & 7) >> 1), l < 6, tag);
        const __amdgpu_buffer_rsrc_t rsO = __builtin_amdgcn_make_buffer_rsrc(ws + WS_OA, 0, S * DM * 2, 0x00020000);
#pragma unroll
        for (int i = 0; i < 6; ++i) t[i] = bload_sc1(rsO, l * 16, (rt * 32 + ((8 * (hg + 1) + 6 * c + i) & 31)) * 1024);
    }
    STAMP(14);
    BAR();
#pragma unroll
    for (int i = 0; i < 6; ++i) *(u32x4*)(lds + ((8 * (hg + 1) + 6 * c + i) & 31) * 1024 + l * 16) = t[i];
    BAR();
    STAMP(15);
    f32x16 acc = {};
    {
        bf16x8 xf[32];
#pragma unroll
        for (int s = 0; s < 32; ++s) xf[s] = *(const bf16x8*)(lds + s * 1024 + l * 16);
#pragma unroll
        for (int s = 0; s < 32; ++s) acc = MFMA32(wa[s], xf[s], acc);
    }
    store_out(acc, A.out, rt, col0, l);
    STAMP(16);
}

__global__ __launch_bounds__(512, 2) void fused_kernel(Args A) {
    extern __shared__ __attribute__((aligned(16))) char lds[];
    STAMP(0);
    const u64 tag = *(const u64*)(A.ws + WS_EPOCH) + 1;
    bf16x8 qa, qb;
    front_phase(A, lds, tag, qa, qb STAMP_PASS);
    if (__builtin_amdgcn_readfirstlane(threadIdx.x >> 6) < 4) attention_wave(A, lds, tag, qa, qb STAMP_PASS);
    else branch_wave(A, lds, tag STAMP_PASS);
    STAMP_FLUSH(24);
}
extern "C" void kernel_launch(void* const* d_in, const int* in_sizes, int n_in, void* d_out, int out_size, void* d_ws, size_t ws_size, hipStream_t stream) {
    Args a;
    a.xa = (const u16*)d_in[0]; a.wq = (const u16*)d_in[1]; a.wk = (const u16*)d_in[2]; a.wv = (const u16*)d_in[3]; a.woa = (const u16*)d_in[4];
    a.xb = (const u16*)d_in[5]; a.wqkv = (const u16*)d_in[6]; a.wob = (const u16*)d_in[7]; a.ws = (char*)d_ws; a.out = (u16*)d_out;
    constexpr int LDSB = 163840;
    (void)hipFuncSetAttribute((const void*)fused_kernel, hipFuncAttributeMaxDynamicSharedMemorySize, LDSB);
    fused_kernel<<<256, 512, LDSB, stream>>>(a);
}
```

```cpp
    "s_mov_b32 s91, 0\n\t"
    "ds_read_b128 %[k], %[ka]\n\t"
    "ds_read_b128 %[v0], %[va]\n\t"
    "v_mov_b32_e32 v252, 0\n\t"
    "v_mov_b32_e32 v253, 0\n\t"
    "v_mov_b32_e32 v254, 0\n\t"
    "v_mov_b32_e32 v255, 0\n\t"
    "s_waitcnt lgkmcnt(0)\n\t"
    "v_mfma_f32_32x32x16_bf16 v[216:231], %[k], %[qa], %[na]\n\t"
    "s_nop 11\n\t"
    "1:\n\t"
    "v_mfma_f32_32x32x16_bf16 v[232:247], %[k], %[qb], %[nb]\n\t"
    "ds_read_b128 %[k], %[ka] offset:1024\n\t"
    "v_exp_f32_e32 v248, v217\n\t"
    "v_exp_f32_e32 v216, v216\n\t"
    "v_exp_f32_e32 v249, v219\n\t"
    "v_exp_f32_e32 v218, v218\n\t"
    "v_mov_b32_sdwa v248, v216 dst_sel:WORD_0 dst_unused:UNUSED_PRESERVE src0_sel:WORD_1\n\t"
    "v_exp_f32_e32 v250, v221\n\t"
    "v_mfma_f32_32x32x16_bf16 %[ob], %[v1], v[252:255], %[ob]\n\t"
    "ds_read_b128 %[v1], %[va] offset:528\n\t"
    "v_exp_f32_e32 v220, v220\n\t"
    "v_mov_b32_sdwa v249, v218 dst_sel:WORD_0 dst_unused:UNUSED_PRESERVE src0_sel:WORD_1\n\t"
    "v_exp_f32_e32 v251, v223\n\t"
    "v_exp_f32_e32 v222, v222\n\t"
    "v_mov_b32_sdwa v250, v220 dst_sel:WORD_0 dst_unused:UNUSED_PRESERVE src0_sel:WORD_1\n\t"
    "v_exp_f32_e32 v252, v225\n\t"
    "v_mov_b32_sdwa v251, v222 dst_sel:WORD_0 dst_unused:UNUSED_PRESERVE src0_sel:WORD_1\n\t"
    "v_exp_f32_e32 v224, v224\n\t"
    "v_exp_f32_e32 v253, v227\n\t"
    "s_waitcnt lgkmcnt(2)\n\t"
    "v_mfma_f32_32x32x16_bf16 %[oa], %[v0], v[248:251], %[oa]\n\t"
    "v_exp_f32_e32 v226, v226\n\t"
    "v_mov_b32_sdwa v252, v224 dst_sel:WORD_0 dst_unused:UNUSED_PRESERVE src0_sel:WORD_1\n\t"
    "v_exp_f32_e32 v254, v229\n\t"
    "v_exp_f32_e32 v228, v228\n\t"
    "v_mov_b32_sdwa v253, v226 dst_sel:WORD_0 dst_unused:UNUSED_PRESERVE src0_sel:WORD_1\n\t"
    "v_exp_f32_e32 v255, v231\n\t"
    "v_exp_f32_e32 v230, v230\n\t"
    "v_mov_b32_sdwa v254, v228 dst_sel:WORD_0 dst_unused:UNUSED_PRESERVE src0_sel:WORD_1\n\t"
    "v_mov_b32_sdwa v255, v230 dst_sel:WORD_0 dst_unused:UNUSED_PRESERVE src0_sel:WORD_1\n\t"
    "s_waitcnt lgkmcnt(1)\n\t"
    "v_mfma_f32_32x32x16_bf16 v[216:231], %[k], %[qa], %[na]\n\t"
    "v_exp_f32_e32 v248, v233\n\t"
    "v_exp_f32_e32 v232, v232\n\t"
    "v_exp_f32_e32 v249, v235\n\t"
    "v_exp_f32_e32 v234, v234\n\t"
    "v_mov_b32_sdwa v248, v232 dst_sel:WORD_0 dst_unused:UNUSED_PRESERVE src0_sel:WORD_1\n\t"
    "v_exp_f32_e32 v250, v237\n\t"
    "s_waitcnt lgkmcnt(0)\n\t"
    "v_mfma_f32_32x32x16_bf16 %[oa], %[v1], v[252:255], %[oa]\n\t"
    "v_exp_f32_e32 v236, v236\n\t"
    "v_mov_b32_sdwa v249, v234 dst_sel:WORD_0 dst_unused:UNUSED_PRESERVE src0_sel:WORD_1\n\t"
    "v_exp_f32_e32 v251, v239\n\t"
    "v_exp_f32_e32 v238, v238\n\t"
    "v_mov_b32_sdwa v250, v236 dst_sel:WORD_0 dst_unused:UNUSED_PRESERVE src0_sel:WORD_1\n\t"
    "v_exp_f32_e32 v252, v241\n\t"
    "v_mov_b32_sdwa v251, v238 dst_sel:WORD_0 dst_unused:UNUSED_PRESERVE src0_sel:WORD_1\n\t"
    "v_exp_f32_e32 v240, v240\n\t"
    "v_exp_f32_e32 v253, v243\n\t"
    "v_mfma_f32_32x32x16_bf16 %[ob], %[v0], v[248:251], %[ob]\n\t"
    "ds_read_b128 %[v0], %[va] offset:1056\n\t"
    "v_exp_f32_e32 v242, v242\n\t"
    "v_mov_b32_sdwa v252, v240 dst_sel:WORD_0 dst_unused:UNUSED_PRESERVE src0_sel:WORD_1\n\t"
    "v_exp_f32_e32 v254, v245\n\t"
    "v_exp_f32_e32 v244, v244\n\t"
    "v_mov_b32_sdwa v253, v242 dst_sel:WORD_0 dst_unused:UNUSED_PRESERVE src0_sel:WORD_1\n\t"
    "v_exp_f32_e32 v255, v247\n\t"
    "v_exp_f32_e32 v246, v246\n\t"
    "v_mov_b32_sdwa v254, v244 dst_sel:WORD_0 dst_unused:UNUSED_PRESERVE src0_sel:WORD_1\n\t"
    "v_mov_b32_sdwa v255, v246 dst_sel:WORD_0 dst_unused:UNUSED_PRESERVE src0_sel:WORD_1\n\t"
    "v_mfma_f32_32x32x16_bf16 v[232:247], %[k], %[qb], %[nb]\n\t"
    "ds_read_b128 %[k], %[ka] offset:2048\n\t"
    "v_exp_f32_e32 v248, v217\n\t"
    "v_exp_f32_e32 v216, v216\n\t"
    "v_exp_f32_e32 v249, v219\n\t"
    "v_exp_f32_e32 v218, v218\n\t"
    "v_mov_b32_sdwa v248, v216 dst_sel:WORD_0 dst_unused:UNUSED_PRESERVE src0_sel:WORD_1\n\t"
    "v_exp_f32_e32 v250, v221\n\t"
    "v_mfma_f32_32x32x16_bf16 %[ob], %[v1], v[252:255], %[ob]\n\t"
    "ds_read_b128 %[v1], %[va] offset:1584\n\t"
    "v_exp_f32_e32 v220, v220\n\t"
    "v_mov_b32_sdwa v249, v218 dst_sel:WORD_0 dst_unused:UNUSED_PRESERVE src0_sel:WORD_1\n\t"
    "v_exp_f32_e32 v251, v223\n\t"
    "v_exp_f32_e32 v222, v222\n\t"
    "v_mov_b32_sdwa v250, v220 dst_sel:WORD_0 dst_unused:UNUSED_PRESERVE src0_sel:WORD_1\n\t"
    "v_exp_f32_e32 v252, v225\n\t"
    "v_mov_b32_sdwa v251, v222 dst_sel:WORD_0 dst_unused:UNUSED_PRESERVE src0_sel:WORD_1\n\t"
    "v_exp_f32_e32 v224, v224\n\t"
    "v_exp_f32_e32 v253, v227\n\t"
    "s_waitcnt lgkmcnt(2)\n\t"
    "v_mfma_f32_32x32x16_bf16 %[oa], %[v0], v[248:251], %[oa]\n\t"
    "v_exp_f32_e32 v226, v226\n\t"
    "v_mov_b32_sdwa v252, v224 dst_sel:WORD_0 dst_unused:UNUSED_PRESERVE src0_sel:WORD_1\n\t"
    "v_exp_f32_e32 v254, v229\n\t"
    "v_exp_f32_e32 v228, v228\n\t"
    "v_mov_b32_sdwa v253, v226 dst_sel:WORD_0 dst_unused:UNUSED_PRESERVE src0_sel:WORD_1\n\t"
    "v_exp_f32_e32 v255, v231\n\t"
    "v_exp_f32_e32 v230, v230\n\t"
    "v_mov_b32_sdwa v254, v228 dst_sel:WORD_0 dst_unused:UNUSED_PRESERVE src0_sel:WORD_1\n\t"
    "v_mov_b32_sdwa v255, v230 dst_sel:WORD_0 dst_unused:UNUSED_PRESERVE src0_sel:WORD_1\n\t"
    "s_waitcnt lgkmcnt(1)\n\t"
    "v_mfma_f32_32x32x16_bf16 v[216:231], %[k], %[qa], %[na]\n\t"
    "v_exp_f32_e32 v248, v233\n\t"
    "v_exp_f32_e32 v232, v232\n\t"
    "v_exp_f32_e32 v249, v235\n\t"
    "v_exp_f32_e32 v234, v234\n\t"
    "v_mov_b32_sdwa v248, v232 dst_sel:WORD_0 dst_unused:UNUSED_PRESERVE src0_sel:WORD_1\n\t"
    "v_exp_f32_e32 v250, v237\n\t"
    "s_waitcnt lgkmcnt(0)\n\t"
    "v_mfma_f32_32x32x16_bf16 %[oa], %[v1], v[252:255], %[oa]\n\t"
    "v_exp_f32_e32 v236, v236\n\t"
    "v_mov_b32_sdwa v249, v234 dst_sel:WORD_0 dst_unused:UNUSED_PRESERVE src0_sel:WORD_1\n\t"
    "v_exp_f32_e32 v251, v239\n\t"
    "v_exp_f32_e32 v238, v238\n\t"
    "v_mov_b32_sdwa v250, v236 dst_sel:WORD_0 dst_unused:UNUSED_PRESERVE src0_sel:WORD_1\n\t"
    "v_exp_f32_e32 v252, v241\n\t"
    "v_mov_b32_sdwa v251, v238 dst_sel:WORD_0 dst_unused:UNUSED_PRESERVE src0_sel:WORD_1\n\t"
    "v_exp_f32_e32 v240, v240\n\t"
    "v_exp_f32_e32 v253, v243\n\t"
    "v_mfma_f32_32x32x16_bf16 %[ob], %[v0], v[248:251], %[ob]\n\t"
    "ds_read_b128 %[v0], %[va] offset:2112\n\t"
    "v_exp_f32_e32 v242, v242\n\t"
    "v_mov_b32_sdwa v252, v240 dst_sel:WORD_0 dst_unused:UNUSED_PRESERVE src0_sel:WORD_1\n\t"
    "v_exp_f32_e32 v254, v245\n\t"
    "v_exp_f32_e32 v244, v244\n\t"
    "v_mov_b32_sdwa v253, v242 dst_sel:WORD_0 dst_unused:UNUSED_PRESERVE src0_sel:WORD_1\n\t"
    "v_exp_f32_e32 v255, v247\n\t"
    "v_exp_f32_e32 v246, v246\n\t"
    "v_mov_b32_sdwa v254, v244 dst_sel:WORD_0 dst_unused:UNUSED_PRESERVE src0_sel:WORD_1\n\t"
    "v_mov_b32_sdwa v255, v246 dst_sel:WORD_0 dst_unused:UNUSED_PRESERVE src0_sel:WORD_1\n\t"
    "v_mfma_f32_32x32x16_bf16 v[232:247], %[k], %[qb], %[nb]\n\t"
    "ds_read_b128 %[k], %[ka] offset:3072\n\t"
    "v_exp_f32_e32 v248, v217\n\t"
    "v_exp_f32_e32 v216, v216\n\t"
    "v_exp_f32_e32 v249, v219\n\t"
    "v_exp_f32_e32 v218, v218\n\t"
    "v_mov_b32_sdwa v248, v216 dst_sel:WORD_0 dst_unused:UNUSED_PRESERVE src0_sel:WORD_1\n\t"
    "v_exp_f32_e32 v250, v221\n\t"
    "v_mfma_f32_32x32x16_bf16 %[ob], %[v1], v[252:255], %[ob]\n\t"
    "ds_read_b128 %[v1], %[va] offset:2640\n\t"
    "v_exp_f32_e32 v220, v220\n\t"
    "v_mov_b32_sdwa v249, v218 dst_sel:WORD_0 dst_unused:UNUSED_PRESERVE src0_sel:WORD_1\n\t"
    "v_exp_f32_e32 v251, v223\n\t"
    "v_exp_f32_e32 v222, v222\n\t"
    "v_mov_b32_sdwa v250, v220 dst_sel:WORD_0 dst_unused:UNUSED_PRESERVE src0_sel:WORD_1\n\t"
    "v_exp_f32_e32 v252, v225\n\t"
    "v_mov_b32_sdwa v251, v222 dst_sel:WORD_0 dst_unused:UNUSED_PRESERVE src0_sel:WORD_1\n\t"
    "v_exp_f32_e32 v224, v224\n\t"
    "v_exp_f32_e32 v253, v227\n\t"
    "s_waitcnt lgkmcnt(2)\n\t"
    "v_mfma_f32_32x32x16_bf16 %[oa], %[v0], v[248:251], %[oa]\n\t"
    "v_exp_f32_e32 v226, v226\n\t"
    "v_mov_b32_sdwa v252, v224 dst_sel:WORD_0 dst_unused:UNUSED_PRESERVE src0_sel:WORD_1\n\t"
    "v_exp_f32_e32 v254, v229\n\t"
    "v_exp_f32_e32 v228, v228\n\t"
    "v_mov_b32_sdwa v253, v226 dst_sel:WORD_0 dst_unused:UNUSED_PRESERVE src0_sel:WORD_1\n\t"
    "v_exp_f32_e32 v255, v231\n\t"
    "v_exp_f32_e32 v230, v230\n\t"
    "v_mov_b32_sdwa v254, v228 dst_sel:WORD_0 dst_unused:UNUSED_PRESERVE src0_sel:WORD_1\n\t"
    "v_mov_b32_sdwa v255, v230 dst_sel:WORD_0 dst_unused:UNUSED_PRESERVE src0_sel:WORD_1\n\t"
    "s_waitcnt lgkmcnt(1)\n\t"
    "v_mfma_f32_32x32x16_bf16 v[216:231], %[k], %[qa], %[na]\n\t"
    "v_exp_f32_e32 v248, v233\n\t"
    "v_exp_f32_e32 v232, v232\n\t"
    "v_exp_f32_e32 v249, v235\n\t"
    "v_exp_f32_e32 v234, v234\n\t"
    "v_mov_b32_sdwa v248, v232 dst_sel:WORD_0 dst_unused:UNUSED_PRESERVE src0_sel:WORD_1\n\t"
    "v_exp_f32_e32 v250, v237\n\t"
    "s_waitcnt lgkmcnt(0)\n\t"
    "v_mfma_f32_32x32x16_bf16 %[oa], %[v1], v[252:255], %[oa]\n\t"
    "v_exp_f32_e32 v236, v236\n\t"
    "v_mov_b32_sdwa v249, v234 dst_sel:WORD_0 dst_unused:UNUSED_PRESERVE src0_sel:WORD_1\n\t"
    "v_exp_f32_e32 v251, v239\n\t"
    "v_exp_f32_e32 v238, v238\n\t"
    "v_mov_b32_sdwa v250, v236 dst_sel:WORD_0 dst_unused:UNUSED_PRESERVE src0_sel:WORD_1\n\t"
    "v_exp_f32_e32 v252, v241\n\t"
    "v_mov_b32_sdwa v251, v238 dst_sel:WORD_0 dst_unused:UNUSED_PRESERVE src0_sel:WORD_1\n\t"
    "v_exp_f32_e32 v240, v240\n\t"
    "v_exp_f32_e32 v253, v243\n\t"
    "v_mfma_f32_32x32x16_bf16 %[ob], %[v0], v[248:251], %[ob]\n\t"
    "ds_read_b128 %[v0], %[va] offset:3168\n\t"
    "v_exp_f32_e32 v242, v242\n\t"
    "v_mov_b32_sdwa v252, v240 dst_sel:WORD_0 dst_unused:UNUSED_PRESERVE src0_sel:WORD_1\n\t"
    "v_exp_f32_e32 v254, v245\n\t"
    "v_exp_f32_e32 v244, v244\n\t"
    "v_mov_b32_sdwa v253, v242 dst_sel:WORD_0 dst_unused:UNUSED_PRESERVE src0_sel:WORD_1\n\t"
    "v_exp_f32_e32 v255, v247\n\t"
    "v_exp_f32_e32 v246, v246\n\t"
    "v_mov_b32_sdwa v254, v244 dst_sel:WORD_0 dst_unused:UNUSED_PRESERVE src0_sel:WORD_1\n\t"
    "v_mov_b32_sdwa v255, v246 dst_sel:WORD_0 dst_unused:UNUSED_PRESERVE src0_sel:WORD_1\n\t"
    "v_mfma_f32_32x32x16_bf16 v[232:247], %[k], %[qb], %[nb]\n\t"
    "ds_read_b128 %[k], %[ka] offset:4096\n\t"
    "v_exp_f32_e32 v248, v217\n\t"
    "v_exp_f32_e32 v216, v216\n\t"
    "v_exp_f32_e32 v249, v219\n\t"
    "v_exp_f32_e32 v218, v218\n\t"
    "v_mov_b32_sdwa v248, v216 dst_sel:WORD_0 dst_unused:UNUSED_PRESERVE src0_sel:WORD_1\n\t"
    "v_exp_f32_e32 v250, v221\n\t"
    "v_mfma_f32_32x32x16_bf16 %[ob], %[v1], v[252:255], %[ob]\n\t"
    "ds_read_b128 %[v1], %[va] offset:3696\n\t"
    "v_exp_f32_e32 v220, v220\n\t"
    "v_mov_b32_sdwa v249, v218 dst_sel:WORD_0 dst_unused:UNUSED_PRESERVE src0_sel:WORD_1\n\t"
    "v_exp_f32_e32 v251, v223\n\t"
    "v_exp_f32_e32 v222, v222\n\t"
    "v_mov_b32_sdwa v250, v220 dst_sel:WORD_0 dst_unused:UNUSED_PRESERVE src0_sel:WORD_1\n\t"
    "v_exp_f32_e32 v252, v225\n\t"
    "v_mov_b32_sdwa v251, v222 dst_sel:WORD_0 dst_unused:UNUSED_PRESERVE src0_sel:WORD_1\n\t"
    "v_exp_f32_e32 v224, v224\n\t"
    "v_exp_f32_e32 v253, v227\n\t"
    "s_waitcnt lgkmcnt(2)\n\t"
    "v_mfma_f32_32x32x16_bf16 %[oa], %[v0], v[248:251], %[oa]\n\t"
    "v_exp_f32_e32 v226, v226\n\t"
    "v_mov_b32_sdwa v252, v224 dst_sel:WORD_0 dst_unused:UNUSED_PRESERVE src0_sel:WORD_1\n\t"
    "v_exp_f32_e32 v254, v229\n\t"
    "v_exp_f32_e32 v228, v228\n\t"
    "v_mov_b32_sdwa v253, v226 dst_sel:WORD_0 dst_unused:UNUSED_PRESERVE src0_sel:WORD_1\n\t"
    "v_exp_f32_e32 v255, v231\n\t"
    "v_exp_f32_e32 v230, v230\n\t"
    "v_mov_b32_sdwa v254, v228 dst_sel:WORD_0 dst_unused:UNUSED_PRESERVE src0_sel:WORD_1\n\t"
    "v_mov_b32_sdwa v255, v230 dst_sel:WORD_0 dst_unused:UNUSED_PRESERVE src0_sel:WORD_1\n\t"
    "s_waitcnt lgkmcnt(1)\n\t"
    "v_mfma_f32_32x32x16_bf16 v[216:231], %[k], %[qa], %[na]\n\t"
    "v_exp_f32_e32 v248, v233\n\t"
    "v_exp_f32_e32 v232, v232\n\t"
    "v_exp_f32_e32 v249, v235\n\t"
    "v_exp_f32_e32 v234, v234\n\t"
    "v_mov_b32_sdwa v248, v232 dst_sel:WORD_0 dst_unused:UNUSED_PRESERVE src0_sel:WORD_1\n\t"
    "v_exp_f32_e32 v250, v237\n\t"
    "s_waitcnt lgkmcnt(0)\n\t"
    "v_mfma_f32_32x32x16_bf16 %[oa], %[v1], v[252:255], %[oa]\n\t"
    "v_exp_f32_e32 v236, v236\n\t"
    "v_mov_b32_sdwa v249, v234 dst_sel:WORD_0 dst_unused:UNUSED_PRESERVE src0_sel:WORD_1\n\t"
    "v_exp_f32_e32 v251, v239\n\t"
    "v_exp_f32_e32 v238, v238\n\t"
    "v_mov_b32_sdwa v250, v236 dst_sel:WORD_0 dst_unused:UNUSED_PRESERVE src0_sel:WORD_1\n\t"
    "v_exp_f32_e32 v252, v241\n\t"
    "v_mov_b32_sdwa v251, v238 dst_sel:WORD_0 dst_unused:UNUSED_PRESERVE src0_sel:WORD_1\n\t"
    "v_exp_f32_e32 v240, v240\n\t"
    "v_exp_f32_e32 v253, v243\n\t"
    "v_mfma_f32_32x32x16_bf16 %[ob], %[v0], v[248:251], %[ob]\n\t"
    "ds_read_b128 %[v0], %[va] offset:4224\n\t"
    "v_exp_f32_e32 v242, v242\n\t"
    "v_mov_b32_sdwa v252, v240 dst_sel:WORD_0 dst_unused:UNUSED_PRESERVE src0_sel:WORD_1\n\t"
    "v_exp_f32_e32 v254, v245\n\t"
    "v_exp_f32_e32 v244, v244\n\t"
    "v_mov_b32_sdwa v253, v242 dst_sel:WORD_0 dst_unused:UNUSED_PRESERVE src0_sel:WORD_1\n\t"
    "v_exp_f32_e32 v255, v247\n\t"
    "v_exp_f32_e32 v246, v246\n\t"
    "s_add_u32 s91, s91, 1\n\t"
    "v_mov_b32_sdwa v254, v244 dst_sel:WORD_0 dst_unused:UNUSED_PRESERVE src0_sel:WORD_1\n\t"
    "v_add_u32_e32 %[ka], 4096, %[ka]\n\t"
    "v_mov_b32_sdwa v255, v246 dst_sel:WORD_0 dst_unused:UNUSED_PRESERVE src0_sel:WORD_1\n\t"
    "v_add_u32_e32 %[va], 4224, %[va]\n\t"
    "s_cmp_lt_u32 s91, %[ns]\n\t"
    "s_cbranch_scc1 1b\n\t"
    "s_waitcnt lgkmcnt(0)\n\t"
    "v_mfma_f32_32x32x16_bf16 %[ob], %[v1], v[252:255], %[ob]\n\t"
    "s_nop 15\n\t"
        : [sc] "=&{v[216:247]}"(sc), [pp] "=&{v[248:255]}"(pp),
          [oa] "+v"(oa), [ob] "+v"(ob), [k] "=&v"(kf), [v0] "+v"(v0), [v1] "+v"(v1), [ka] "+v"(ka), [va] "+v"(va)
        : [qa] "v"(qa), [qb] "v"(qb), [na] "v"(na), [nb] "v"(nb), [ns] "s"(ns)
        : "memory", "scc", "s91");
}
DI float halfmax(float m) {
    auto sw = __builtin_amdgcn_permlane32_swap(__float_as_uint(m), __float_as_uint(m), false, false);
    return fmaxf(__uint_as_float(sw[0]), __uint_as_float(sw[1]));
}
DI float max16(const f32x16& s) {
    float m = s[0];
#pragma unroll
    for (int i = 1; i < 16; ++i) m = fmaxf(m, s[i]);
    return m;
}
DI float nonfinite_probe(const f32x16& o, float t) {
#pragma unroll
    for (int i = 0; i < 9; ++i) t = __builtin_fmaf(o[i], 0.f, t);
    return t;
}

DI void attention_wave(const Args& A, char* lds, u64 tag, const bf16x8 qa, const bf16x8 qb STAMP_ARG) {
    char* ws = A.ws;
    const int tid = threadIdx.x, l = tid & 63, c = __builtin_amdgcn_readfirstlane(tid >> 6);
    const int h = l >> 5, r = l & 31;
    const Ids id = ids_of(blockIdx.x);
    const unsigned L0 = lds_addr(lds);
    const unsigned kaddr = L0 + l * 16;
    const unsigned vaddr = r < 16 ? L0 + 65536 + h * 256 + r * 16 : ((r == 16 || r == 20) ? L0 + 65536 + 512 : 0x100000u);
    float ma, mb;
    {
        const f32x16 zero = {};
        const bf16x8 k0 = *(const bf16x8*)(lds + l * 16);
        ma = halfmax(max16(MFMA32(k0, qa, zero))) + MSHIFT; mb = halfmax(max16(MFMA32(k0, qb, zero))) + MSHIFT;
    }
    f32x16 oa, ob;
    STAMP(6);
#pragma unroll 1
    for (int attempt = 0; attempt < 2; ++attempt) {
        att_pass(qa, qb, ma, mb, kaddr, vaddr, oa, ob);
        STAMP(7);
        const float probe = nonfinite_probe(ob, nonfinite_probe(oa, 0.f));
        if (attempt == 1 || __all(probe == 0.f)) break;
        const f32x16 zero = {};
        float xa = -INFINITY, xb = -INFINITY;
#pragma unroll 1
        for (int t = 0; t < 64; ++t) {
            const bf16x8 kt = *(const bf16x8*)(lds + l * 16 + t * 1024);
            xa = fmaxf(xa, max16(MFMA32(kt, qa, zero))); xb = fmaxf(xb, max16(MFMA32(kt, qb, zero)));
        }
        ma = halfmax(xa); mb = halfmax(xb);
    }
    const u32x4 ova = pack8(oa, 0, __builtin_amdgcn_rcpf(oa[8])), ovb = pack8(ob, 0, __builtin_amdgcn_rcpf(ob[8]));
    const __amdgpu_buffer_rsrc_t rsO = __builtin_amdgcn_make_buffer_rsrc(ws + WS_OA, 0, S * DM * 2, 0x00020000);
    const int fr = 8 * id.hg + 2 * c;
    bstore_sc1(ova, rsO, l * 16, (id.rt * 32 + fr) * 1024); bstore_sc1(ovb, rsO, l * 16, (id.rt * 32 + fr + 1) * 1024);
    WAIT_VM(0);
    if (l == 0) __hip_atomic_store((GLBP u64*)(ws + WS_FLAG) + 1536 + (id.rt * 4 + id.hg) * 4 + c, tag, RLX_AGENT);
    STAMP(8);
    BAR();
    *(u32x4*)(lds + fr * 1024 + l * 16) = ova; *(u32x4*)(lds + (fr + 1) * 1024 + l * 16) = ovb;
    BAR();
    STAMP(9);
}

template <typename G, typename F>
DI void stream_w(const u16* wo, int col0, char* ring, unsigned ringL, int l, G&& after_first_issue, F&& consume) {
    const int h = l >> 5, blk = (l >> 4) & 1, q4 = (l & 15) >> 2, p4 = l & 3, sg = ((p4 & 1) << 1) | (p4 >> 1);
    const unsigned voff = ((l >> 2) * 512 + col0 + (l & 3) * 8) * 2;
    const char* rd = ring + (8 * h + q4) * 64 + blk * 32 + sg * 8;
    auto issue = [&](int ch) {
        glds16s((const char*)wo + (size_t)ch * 32 * 1024, voff, ringL + (ch % 3) * 2048);
        glds16s((const char*)wo + (size_t)ch * 32 * 1024 + 16 * 1024, voff, ringL + (ch % 3) * 2048 + 1024);
    };
    issue(0); issue(1); issue(2);
    after_first_issue();
#pragma unroll
    for (int ch = 0; ch < 16; ++ch) {
        if (ch <= 13) { WAIT_VM(4); } else if (ch == 14) { WAIT_VM(2); } else { WAIT_VM(0); }
        const char* p = rd + (ch % 3) * 2048;
        const bf16x8 f0 = tr_frag(p, p + 256), f1 = tr_frag(p + 1024, p + 1024 + 256);
        asm volatile("s_waitcnt lgkmcnt(0)" : "+v"(const_cast<bf16x8&>(f0)), "+v"(const_cast<bf16x8&>(f1)) :: "memory");
        if (ch + 3 < 16) issue(ch + 3);
        consume(2 * ch, f0); consume(2 * ch + 1, f1);
    }
}
DI void store_out(const f32x16& acc, u16* out, int rt, int col, int l) {
    u16* dst = out + (size_t)(32 * rt + (l & 31)) * 1024 + col + 8 * (l >> 5);
    *(u32x4*)dst = pack8(acc, 0, 1.f); *(u32x4*)(dst + 16) = pack8(acc, 8, 1.f);
}

DI void branch_wave(const Args& A, char* lds, u64 tag STAMP_ARG) {
    char* ws = A.ws;
    const int tid = threadIdx.x, l = tid & 63, c = __builtin_amdgcn_readfirstlane(tid >> 6) - 4;
    const Ids id = ids_of(blockIdx.x);
    const int rt = id.rt, hg = id.hg, brt = id.brt, hq = id.hq;
    const unsigned L0 = lds_addr(lds);
    GLBP u64* flags = (GLBP u64*)(ws + WS_FLAG);
    const __amdgpu_buffer_rsrc_t rsB = __builtin_amdgcn_make_buffer_rsrc(ws + WS_OB, 0, S * DM * 2, 0x00020000);
    __builtin_amdgcn_s_setprio(BPRIO);
    {
        const char* qt = lds + QT;
        const int lr = 16 * c + (l >> 2), hd = l & 3, grow = 64 * brt + lr;
        auto ld = [&](int row, int chunk) { return *(const u32x4*)(qt + row * 384 + ((chunk ^ (row & 7)) * 16)); };
        const u32x4 q0 = ld(16 + lr, 2 * hd), q1 = ld(16 + lr, 2 * hd + 1);
        float lg[8];
#pragma unroll
        for (int wv = 0; wv < 8; ++wv) {
            const u32x4 k0 = ld(16 + lr - 2 * wv, 8 + 2 * hd), k1 = ld(16 + lr - 2 * wv, 8 + 2 * hd + 1);
            float s0 = 0.f, s1 = 0.f;
#pragma unroll
            for (int i = 0; i < 4; ++i) { s0 = dot2(q0[i], k0[i], s0); s1 = dot2(q1[i], k1[i], s1); }
            lg[wv] = (grow - 2 * wv < 0) ? -INFINITY : (s0 + s1) * QSCALE;
        }
        float m = lg[0];
#pragma unroll
        for (int wv = 1; wv < 8; ++wv) m = fmaxf(m, lg[wv]);
        float pw[8], ls = 0.f;
#pragma unroll
        for (int wv = 0; wv < 8; ++wv) { pw[wv] = __builtin_amdgcn_exp2f(lg[wv] - m); ls += pw[wv]; }
        const float inv = __builtin_amdgcn_rcpf(ls);
        float o[16];
#pragma unroll
        for (int i = 0; i < 16; ++i) o[i] = 0.f;
#pragma unroll
        for (int wp = 0; wp < 4; ++wp) {
            const unsigned pp = pk_bf16(pw[2 * wp] * inv, pw[2 * wp + 1] * inv);
            const int ra = 16 + lr - 4 * wp, rb = ra - 2;
            const u32x4 a0 = ld(ra, 16 + 2 * hd), a1 = ld(ra, 16 + 2 * hd + 1), c0 = ld(rb, 16 + 2 * hd), c1 = ld(rb, 16 + 2 * hd + 1);
#pragma unroll
            for (int i = 0; i < 4; ++i) {
                o[2 * i] = dot2(__builtin_amdgcn_perm(c0[i], a0[i], 0x05040100u), pp, o[2 * i]);
                o[2 * i + 1] = dot2(__builtin_amdgcn_perm(c0[i], a0[i], 0x07060302u), pp, o[2 * i + 1]);
                o[8 + 2 * i] = dot2(__builtin_amdgcn_perm(c1[i], a1[i], 0x05040100u), pp, o[8 + 2 * i]);
                o[8 + 2 * i + 1] = dot2(__builtin_amdgcn_perm(c1[i], a1[i], 0x07060302u), pp, o[8 + 2 * i + 1]);
            }
        }
        u32x4 r0, r1;
#pragma unroll
        for (int i = 0; i < 4; ++i) { r0[i] = pk_bf16(o[2 * i], o[2 * i + 1]); r1[i] = pk_bf16(o[8 + 2 * i], o[8 + 2 * i + 1]); }
        const int off = ((grow >> 5) * 32 + 4 * hq + hd) * 1024 + (grow & 31) * 16;
        bstore_sc1(r0, rsB, off, 0); bstore_sc1(r1, rsB, off + 512, 0);
    }
    WAIT_VM(0);
    if (l == 0) __hip_atomic_store(flags + 512 + (8 * brt + hq) * 4 + c, tag, RLX_AGENT);
    STAMP(10);
    poll_flags(l < 32 ? flags + 512 + 32 * (rt >> 1) + l : flags + 512 + (8 * brt + hq) * 4 + (l & 3), l < 36, tag);
    STAMP(11);
    char* ring = lds + QT + c * 7680; const unsigned ringL = L0 + QT + c * 7680;
    const int col0 = 128 * hg + 32 * c;
    {
        u32x4 xb[32];
        f32x16 acc = {};
        stream_w(A.wob, col0, ring, ringL, l, [&]() {
#pragma unroll
            for (int s = 0; s < 32; ++s) xb[s] = bload_sc1(rsB, l * 16, (rt * 32 + s) * 1024);
#pragma unroll
            for (int s = 0; s < 32; ++s) asm volatile("" : "+v"(xb[s]));
        }, [&](int ks, const bf16x8& f) { acc = MFMA32(f, __builtin_bit_cast(bf16x8, xb[ks]), acc); });
        store_out(acc, A.out, rt, 512 + col0, l);
    }
    STAMP(12);
    bf16x8 wa[32];
    stream_w(A.woa, col0, ring, ringL, l, [&]() {}, [&](int ks, const bf16x8& f) { wa[ks] = f; });
    STAMP(13);
    __builtin_amdgcn_s_setprio(0);
    BAR();
    u32x4 t[6];
    {
        const int f = (8 * (hg + 1) + 6 * c + (l < 6 ? l : 0)) & 31;
        poll_flags(flags + 1536 + (rt * 4 + (f >> 3)) * 4 + ((f & 7) >> 1), l < 6, tag);
        const __amdgpu_buffer_rsrc_t rsO = __builtin_amdgcn_make_buffer_rsrc(ws + WS_OA, 0, S * DM * 2, 0x00020000);
#pragma unroll
        for (int i = 0; i < 6; ++i) t[i] = bload_sc1(rsO, l * 16, (rt * 32 + ((8 * (hg + 1) + 6 * c + i) & 31)) * 1024);
    }
    STAMP(14);
#pragma unroll
    for (int i = 0; i < 6; ++i) *(u32x4*)(lds + ((8 * (hg + 1) + 6 * c + i) & 31) * 1024 + l * 16) = t[i];
    BAR();
    STAMP(15);
    f32x16 acc = {};
    {
        bf16x8 xf[32];
#pragma unroll
        for (int s = 0; s < 32; ++s) xf[s] = *(const bf16x8*)(lds + s * 1024 + l * 16);
#pragma unroll
        for (int s = 0; s < 32; ++s) acc = MFMA32(wa[s], xf[s], acc);
    }
    store_out(acc, A.out, rt, col0, l);
    STAMP(16);
}

__global__ __launch_bounds__(512, 2) void fused_kernel(Args A) {
    extern __shared__ __attribute__((aligned(16))) char lds[];
    STAMP(0);
    const u64 tag = *(const u64*)(A.ws + WS_EPOCH) + 1;
    if (__builtin_amdgcn_readfirstlane(threadIdx.x >> 6) < 4) {
        bf16x8 qa, qb;
        front_compute(A, lds, tag, qa, qb STAMP_PASS);
        attention_wave(A, lds, tag, qa, qb STAMP_PASS);
    } else {
        front_stage(A, lds, tag STAMP_PASS);
        branch_wave(A, lds, tag STAMP_PASS);
    }
    STAMP_FLUSH(24);
}
extern "C" void kernel_launch(void* const* d_in, const int* in_sizes, int n_in, void* d_out, int out_size, void* d_ws, size_t ws_size, hipStream_t stream) {
    Args a;
    a.xa = (const u16*)d_in[0]; a.wq = (const u16*)d_in[1]; a.wk = (const u16*)d_in[2]; a.wv = (const u16*)d_in[3]; a.woa = (const u16*)d_in[4];
    a.xb = (const u16*)d_in[5]; a.wqkv = (const u16*)d_in[6]; a.wob = (const u16*)d_in[7]; a.ws = (char*)d_ws; a.out = (u16*)d_out;
    constexpr int LDSB = 163840;
    (void)hipFuncSetAttribute((const void*)fused_kernel, hipFuncAttributeMaxDynamicSharedMemorySize, LDSB);
    fused_kernel<<<256, 512, LDSB, stream>>>(a);
}
```

```cpp
#include <hip/hip_runtime.h>
#include <hip/hip_bf16.h>
#include <stdint.h>

typedef short bf16x8 __attribute__((ext_vector_type(8)));
typedef short s16x4 __attribute__((ext_vector_type(4)));
typedef float f32x16 __attribute__((ext_vector_type(16)));
typedef float f32x32 __attribute__((ext_vector_type(32)));
typedef float f32x4 __attribute__((ext_vector_type(4)));
typedef unsigned u32x4 __attribute__((ext_vector_type(4)));
typedef unsigned u32x8 __attribute__((ext_vector_type(8)));
typedef unsigned u32x2 __attribute__((ext_vector_type(2)));
typedef unsigned short u16;
typedef unsigned long long u64;
#define LDSP __attribute__((address_space(3)))
#define GLBP __attribute__((address_space(1)))
#define DI __device__ __forceinline__
#define MFMA32(a, b, c) __builtin_amdgcn_mfma_f32_32x32x16_bf16((a), (b), (c), 0, 0, 0)
#define MFMA16(a, b, c) __builtin_amdgcn_mfma_f32_16x16x32_bf16((a), (b), (c), 0, 0, 0)
#define WAIT_VM(n) asm volatile("s_waitcnt vmcnt(" #n ")" ::: "memory")
#define BAR() do { asm volatile("s_waitcnt lgkmcnt(0)" ::: "memory"); __builtin_amdgcn_s_barrier(); asm volatile("" ::: "memory"); } while (0)
#define RLX_AGENT __ATOMIC_RELAXED, __HIP_MEMORY_SCOPE_AGENT

constexpr int S = 2048, DM = 512;
constexpr float QSCALE = 0.25f * 1.4426950408889634f;
constexpr float MSHIFT = 90.f;
constexpr int SLOT = 32768;
constexpr int VGRP = 528;
constexpr int KV_BYTES = 65536 + 128 * VGRP;
constexpr int QT = KV_BYTES;
constexpr size_t WS_KV = 0;
constexpr size_t WS_FLAG = 136u << 10;
constexpr size_t WS_EPOCH = WS_FLAG + 3584 * 8;
constexpr size_t WS_OB = 256u << 10;
constexpr size_t WS_OA = WS_OB + (2u << 20);

typedef __bf16 bf16x2_t __attribute__((ext_vector_type(2)));
typedef float f32x2_t __attribute__((ext_vector_type(2)));
DI unsigned pk_bf16(float lo, float hi) { f32x2_t v = {lo, hi}; return __builtin_bit_cast(unsigned, __builtin_convertvector(v, bf16x2_t)); }
DI void glds16(const void* g, unsigned lb) {
    unsigned sv;
    asm volatile("s_mov_b32 %0, m0\n\ts_mov_b32 m0, %2\n\ts_nop 0\n\tglobal_load_lds_dwordx4 %1, off\n\ts_mov_b32 m0, %0" : "=&s"(sv) : "v"(g), "s"(lb) : "memory");
}
DI void glds16x4(const char* sbase, unsigned v0, unsigned v1, unsigned v2, unsigned v3, unsigned lb) {
    asm volatile("s_mov_b32 m0, %5\n\ts_nop 0\n\tglobal_load_lds_dwordx4 %0, %4\n\tglobal_load_lds_dwordx4 %1, %4 offset:1024\n\tglobal_load_lds_dwordx4 %2, %4 offset:2048\n\tglobal_load_lds_dwordx4 %3, %4 offset:3072"
                 :: "v"(v0), "v"(v1), "v"(v2), "v"(v3), "s"(sbase), "s"(lb) : "memory");
}
DI void glds16x2(const char* sbase, unsigned v0, unsigned v1, unsigned lb) {
    asm volatile("s_mov_b32 m0, %3\n\ts_nop 0\n\tglobal_load_lds_dwordx4 %0, %2\n\tglobal_load_lds_dwordx4 %1, %2 offset:1024" :: "v"(v0), "v"(v1), "s"(sbase), "s"(lb) : "memory");
}
DI void glds16_sc1(const void* g, unsigned lb) {
    unsigned sv;
    asm volatile("s_mov_b32 %0, m0\n\ts_mov_b32 m0, %2\n\ts_nop 0\n\tglobal_load_lds_dwordx4 %1, off sc1\n\ts_mov_b32 m0, %0" : "=&s"(sv) : "v"(g), "s"(lb) : "memory");
}
DI void glds16x4_sc1(const char* sbase, unsigned voff, unsigned lb) {
    asm volatile("s_mov_b32 m0, %2\n\ts_nop 0\n\tglobal_load_lds_dwordx4 %0, %1 sc1\n\tglobal_load_lds_dwordx4 %0, %1 offset:1024 sc1\n\tglobal_load_lds_dwordx4 %0, %1 offset:2048 sc1\n\tglobal_load_lds_dwordx4 %0, %1 offset:3072 sc1"
                 :: "v"(voff), "s"(sbase), "s"(lb) : "memory");
}
DI unsigned lds_addr(const char* lds) { return __builtin_amdgcn_readfirstlane((unsigned)(uintptr_t)(const LDSP char*)lds); }
typedef short v4i16_t __attribute__((ext_vector_type(4)));
DI s16x4 tr_read(const char* p) { return __builtin_bit_cast(s16x4, __builtin_amdgcn_ds_read_tr16_b64_v4i16((LDSP v4i16_t*)p)); }
DI bf16x8 tr_frag(const char* p0, const char* p1) { const s16x4 lo = tr_read(p0), hi = tr_read(p1); return __builtin_shufflevector(lo, hi, 0, 1, 2, 3, 4, 5, 6, 7); }
DI float dot2(unsigned a, unsigned b, float c) { return __builtin_amdgcn_fdot2_f32_bf16(__builtin_bit_cast(bf16x2_t, a), __builtin_bit_cast(bf16x2_t, b), c, false); }
DI u32x4 bload_sc1(__amdgpu_buffer_rsrc_t rs, int voff, int soff) { return __builtin_bit_cast(u32x4, __builtin_amdgcn_raw_buffer_load_b128(rs, voff, soff, 16)); }
DI void bstore_sc1(u32x4 v, __amdgpu_buffer_rsrc_t rs, int voff, int soff) { __builtin_amdgcn_raw_buffer_store_b128(v, rs, voff, soff, 16); }
DI u32x4 pack8(const f32x16& a, int o, float s) {
    u32x4 v;
#pragma unroll
    for (int j = 0; j < 4; ++j) v[j] = pk_bf16(a[o + 2 * j] * s, a[o + 2 * j + 1] * s);
    return v;
}
DI float halfmax(float m) {
    auto sw = __builtin_amdgcn_permlane32_swap(__float_as_uint(m), __float_as_uint(m), false, false);
    return fmaxf(__uint_as_float(sw[0]), __uint_as_float(sw[1]));
}
DI float max16(const f32x16& s) {
    float m = s[0];
#pragma unroll
    for (int i = 1; i < 16; ++i) m = fmaxf(m, s[i]);
    return m;
}
DI void poll_flags(const GLBP u64* f, bool act, u64 tag) {
#pragma unroll 1
    for (unsigned spins = 0; spins < (1u << 20); ++spins) {
        const bool ok = !act || __hip_atomic_load(f, RLX_AGENT) == tag;
        if (__all(ok)) break;
        __builtin_amdgcn_s_sleep(1);
    }
}

struct Ids { int rt, hg, brt, hq; };
DI Ids ids_of(int b) { const int x = b & 7, i = b >> 3; Ids d; d.rt = 16 * (x >> 1) + (i >> 1); d.hg = 2 * (x & 1) + (i & 1); d.brt = 8 * (x >> 1) + (i >> 2); d.hq = 4 * (x & 1) + (i & 3); return d; }
struct Args { const u16 *xa, *wq, *wk, *wv, *woa, *xb, *wqkv, *wob; char* ws; u16* out; };
DI void front_compute(const Args& A, char* lds, u64 tag, bf16x8& qa, bf16x8& qb, float& ma, float& mb) {
    char* ws = A.ws;
    const int tid = threadIdx.x, l = tid & 63, w = __builtin_amdgcn_readfirstlane(tid >> 6);
    const int h = l >> 5, r = l & 31;
    const int b = blockIdx.x;
    const Ids id = ids_of(b);
    const int rt = id.rt, hg = id.hg;
    const int blk = (l >> 4) & 1, q4 = (l & 15) >> 2, p4 = l & 3, sg = ((p4 & 1) << 1) | (p4 >> 1);
    const unsigned L0 = lds_addr(lds);
    GLBP u64* flags = (GLBP u64*)(ws + WS_FLAG);
    const int rotq = (b >> 4) & 3;
    const int rotb = ((b >> 5) + 2 * ((b >> 3) & 3)) & 7;
    {
        const u16* wkv = (hg >> 1) ? A.wv : A.wk;
#pragma unroll
        for (int i = 0; i < 4; ++i) glds16(wkv + (128 * ((w + rotq) & 3) + 32 * i + (l >> 1)) * 16 + (l & 1) * 8, L0 + 4 * SLOT + w * 4096 + i * 1024);
    }
    char* s4 = lds + 4 * SLOT;
    WAIT_VM(0);
    f32x16 qacc = {};
    int wqoff[2], xo[8];
#pragma unroll
    for (int t = 0; t < 2; ++t) { const int kr = 8 * h + 4 * t + q4; const int chunk = 4 * w + 2 * blk + (sg >> 1); wqoff[t] = kr * 256 + ((chunk ^ (((kr & 3) << 2) | ((kr >> 2) & 3))) * 16) + 8 * (sg & 1); }
#pragma unroll
    for (int s = 0; s < 8; ++s) xo[s] = r * 256 + (((2 * s + h) ^ (r & 15)) * 16);
    bf16x8 fx[2][8], fw[2][8];
    auto qload = [&](int set, int it) {
        const char* sl = lds + it * SLOT; const char* xl = lds + (it - 1) * 8192;
#pragma unroll
        for (int s = 0; s < 8; ++s) { fx[set][s] = *(const bf16x8*)(xl + xo[s]); fw[set][s] = tr_frag(sl + wqoff[0] + (s >> 2) * 16384 + (s & 3) * 16 * 256, sl + wqoff[1] + (s >> 2) * 16384 + (s & 3) * 16 * 256); }
    };
    auto qmma = [&](int set) {
#pragma unroll
        for (int s = 0; s < 8; ++s) qacc = MFMA32(fw[set][s], fx[set][s], qacc);
    };
    BAR();
    unsigned pf1, pf2;
    {
        const int xi = b >> 3, xq = (b & 7) >> 1, xh = b & 1;
        const char* p1 = (w < 3) ? (const char*)(A.wqkv + (size_t)(16 * xi + (l >> 2)) * 1536 + w * 512 + xh * 256) + (l & 3) * 128
                                 : (const char*)(A.xb + (size_t)(512 * xq + 16 * xi) * DM) + l * 128;
        int hr = 512 * xq - 16 + (l >> 3); hr = hr < 0 ? 0 : hr;
        const char* p2 = (w == 3) ? (const char*)(A.xb + (size_t)(512 * xq + 16 * xi) * DM) + (64 + l) * 128
                                  : ((w < 2 && xi == 0) ? (const char*)(A.xb + (size_t)(hr + 8 * w) * DM) + (l & 7) * 128 : p1);
        asm volatile("global_load_dword %0, %2, off\n\tglobal_load_dword %1, %3, off" : "=&v"(pf1), "=&v"(pf2) : "v"(p1), "v"(p2) : "memory");
    }
    {
        f32x4 acc = {};
        const int g = l >> 4, row = 16 * (hg & 1) + (l & 15);
#pragma unroll
        for (int s = 0; s < 4; ++s) {
            const char* a = s4 + w * 4096 + (32 * s + 8 * g + q4) * 32 + p4 * 8;
            const bf16x8 wf = tr_frag(a, a + 4 * 32);
            const bf16x8 xf = *(const bf16x8*)(lds + w * 8192 + row * 256 + (((4 * s + g) ^ (row & 15)) * 16));
            acc = (hg >> 1) ? MFMA16(xf, wf, acc) : MFMA16(wf, xf, acc);
        }
        *(f32x4*)(s4 + w * 4096 + l * 16) = acc;
    }
    qload(0, 1);
    BAR();
    if (w == 0) {
        const int g = l >> 4;
        f32x4 sum = *(const f32x4*)(s4 + l * 16);
#pragma unroll
        for (int i = 1; i < 4; ++i) sum += *(const f32x4*)(s4 + i * 4096 + l * 16);
        const u64 pk = (u64)pk_bf16(sum[0], sum[1]) | ((u64)pk_bf16(sum[2], sum[3]) << 32);
        const int i16 = l & 15;
        if (hg >> 1) {
            const int rho = 8 * ((i16 >> 2) & 1) + 4 * (i16 >> 3) + (i16 & 3);
            char* vg = ws + WS_KV + 65536 + (2 * rt + (hg & 1)) * VGRP;
            __hip_atomic_store((GLBP u64*)(vg + (g & 1) * 256 + rho * 16 + (g >> 1) * 8), pk, RLX_AGENT);
            if (l < 2) __hip_atomic_store((GLBP u64*)(vg + 512 + l * 8), 0x3f803f803f803f80ull, RLX_AGENT);
        } else {
            __hip_atomic_store((GLBP u64*)(ws + WS_KV + rt * 1024 + (g >> 1) * 512 + (16 * (hg & 1) + i16) * 16 + (g & 1) * 8), pk, RLX_AGENT);
        }
    }
    qload(1, 2); qmma(0);
    asm volatile("s_waitcnt vmcnt(0)" : "+v"(pf1), "+v"(pf2) :: "memory");
    BAR();
    if (w == 0) { WAIT_VM(0); if (l == 0) __hip_atomic_store(flags + b, tag, RLX_AGENT); }
    qload(0, 3); qmma(1);
    BAR();
    qload(1, 4); qmma(0);
    qmma(1);
    qa = __builtin_bit_cast(bf16x8, pack8(qacc, 0, QSCALE)); qb = __builtin_bit_cast(bf16x8, pack8(qacc, 8, QSCALE));
    asm volatile("" : "+v"(qa), "+v"(qb));
    BAR();
    f32x16 bacc[4] = {};
    const int sw = (q4 >> 1) & 1;
    const int wbase = 8192 + (8 * h + q4) * 384 + blk * 32 + sg * 8;
    if (w < 3) {
        int xo0[4];
#pragma unroll
        for (int j = 0; j < 4; ++j) xo0[j] = r * 128 + (((2 * j + h) ^ ((r >> 1) & 7)) * 16);
        const int wo0 = wbase + ((2 * w) ^ sw) * 64, wo1 = wbase + ((2 * w + 1) ^ sw) * 64;
        bf16x8 gx[2][4][2], gw[2][4][2];
        u32x4 fl0 = {0, 0, 0, 0}, fl1 = {0, 0, 0, 0}; const unsigned tlo = (unsigned)tag, thi = (unsigned)(tag >> 32);
        auto bload = [&](int set, int s) {
            const char* sl = lds + ((s & 3) + 1) * SLOT;
#pragma unroll
            for (int j = 0; j < 4; ++j) {
                gx[set][j][0] = *(const bf16x8*)(sl + xo0[j]); gx[set][j][1] = *(const bf16x8*)(sl + 4096 + xo0[j]);
                gw[set][j][0] = tr_frag(sl + wo0 + j * 16 * 384, sl + wo0 + j * 16 * 384 + 4 * 384);
                gw[set][j][1] = tr_frag(sl + wo1 + j * 16 * 384, sl + wo1 + j * 16 * 384 + 4 * 384);
            }
        };
        bload(0, 0);
#pragma unroll 1
        for (int s2 = 0; s2 < 8; s2 += 2) {
#pragma unroll
            for (int u = 0; u < 2; ++u) {
                if (w == 0 && s2 == 2 && u == 1) {
                    asm volatile("s_waitcnt vmcnt(0)" : "+v"(fl0), "+v"(fl1) :: "memory");
                    bool ok = fl0[0] == tlo && fl0[1] == thi && fl0[2] == tlo && fl0[3] == thi && fl1[0] == tlo && fl1[1] == thi && fl1[2] == tlo && fl1[3] == thi;
#pragma unroll 1
                    for (unsigned spins = 0; !__all(ok) && spins < (1u << 20); ++spins) {
                        __builtin_amdgcn_s_sleep(1);
                        const GLBP u64* f = flags + 4 * l;
                        const u64 f0 = __hip_atomic_load(f, RLX_AGENT), f1 = __hip_atomic_load(f + 1, RLX_AGENT), f2 = __hip_atomic_load(f + 2, RLX_AGENT), f3 = __hip_atomic_load(f + 3, RLX_AGENT);
                        ok = ((f0 ^ tag) | (f1 ^ tag) | (f2 ^ tag) | (f3 ^ tag)) == 0;
                    }
                    if (b == 0 && l == 0) __hip_atomic_store((GLBP u64*)(ws + WS_EPOCH), tag, RLX_AGENT);
                }
                BAR();
                if (s2 == 0 && u == 1)
                    asm volatile("global_load_dwordx4 %0, %2, off sc1\n\tglobal_load_dwordx4 %1, %2, off offset:16 sc1" : "=&v"(fl0), "=&v"(fl1) : "v"(flags + 4 * l) : "memory");
                bload(u ^ 1, s2 + u + 1);
#pragma unroll
                for (int j = 0; j < 4; ++j)
#pragma unroll
                    for (int t = 0; t < 4; ++t) bacc[t] = MFMA32(gw[u][j][t & 1], gx[u][j][t >> 1], bacc[t]);
#pragma unroll
                for (int g = 0; g < 12; ++g) { __builtin_amdgcn_sched_group_barrier(0x8, 1, 0); __builtin_amdgcn_sched_group_barrier(0x100, 2, 0); }
                __builtin_amdgcn_sched_group_barrier(0x8, 4, 0);
            }
        }
        asm volatile("s_waitcnt vmcnt(0)" : "+v"(fl0), "+v"(fl1) :: "memory");
    } else {
        int xh[4], wo[4];
#pragma unroll
        for (int j = 0; j < 4; ++j) { xh[j] = (r & 15) * 1024 + (((2 * j + h) ^ (r & 15)) * 16); wo[j] = wbase + ((2 + j) ^ sw) * 64; }
        bf16x8 gx[2][4], gw[2][4][4];
        auto hload = [&](int set, int s) {
            const char* sl = lds + ((s & 3) + 1) * SLOT; const int kc = (s + rotb) & 7;
#pragma unroll
            for (int j = 0; j < 4; ++j) {
                gx[set][j] = *(const bf16x8*)(lds + ((xh[j] ^ (128 * (kc & 1))) + (kc >> 1) * 256));
#pragma unroll
                for (int t = 0; t < 4; ++t) gw[set][j][t] = tr_frag(sl + wo[t] + j * 16 * 384, sl + wo[t] + j * 16 * 384 + 4 * 384);
            }
        };
        hload(0, 0);
#pragma unroll 1
        for (int s2 = 0; s2 < 8; s2 += 2) {
#pragma unroll
            for (int u = 0; u < 2; ++u) {
                BAR();
                hload(u ^ 1, s2 + u + 1);
#pragma unroll
                for (int j = 0; j < 4; ++j)
#pragma unroll
                    for (int t = 0; t < 4; ++t) bacc[t] = MFMA32(gw[u][j][t], gx[u][j], bacc[t]);
#pragma unroll
                for (int g = 0; g < 12; ++g) { __builtin_amdgcn_sched_group_barrier(0x8, 1, 0); __builtin_amdgcn_sched_group_barrier(0x100, 3, 0); }
                __builtin_amdgcn_sched_group_barrier(0x8, 4, 0);
            }
        }
    }
    char* qt = lds + QT;
    BAR();
    if (w < 3) {
#pragma unroll
        for (int t = 0; t < 4; ++t) {
            const int row = 16 + 32 * (t >> 1) + r, ct = 2 * w + (t & 1), x7 = row & 7;
            char* d = qt + row * 384;
            *(u32x4*)(d + (((4 * ct + h) ^ x7) * 16)) = pack8(bacc[t], 0, 1.f); *(u32x4*)(d + (((4 * ct + 2 + h) ^ x7) * 16)) = pack8(bacc[t], 8, 1.f);
        }
    } else if (r < 16) {
#pragma unroll
        for (int t = 0; t < 4; ++t) {
            const int ct = 2 + t, x7 = r & 7;
            char* d = qt + r * 384;
            *(u32x4*)(d + (((4 * ct + h) ^ x7) * 16)) = pack8(bacc[t], 0, 1.f); *(u32x4*)(d + (((4 * ct + 2 + h) ^ x7) * 16)) = pack8(bacc[t], 8, 1.f);
        }
    }
    {
        const f32x16 zero = {};
        const bf16x8 k0 = *(const bf16x8*)(lds + 32 * 1024 + l * 16);
        ma = halfmax(max16(MFMA32(k0, qa, zero))) + MSHIFT; mb = halfmax(max16(MFMA32(k0, qb, zero))) + MSHIFT;
        asm volatile("" : "+v"(ma), "+v"(mb));
    }
    BAR();
}

DI void front_stage(const Args& A, char* lds, u64 tag) {
    char* ws = A.ws;
    const int tid = threadIdx.x, l = tid & 63, w = __builtin_amdgcn_readfirstlane(tid >> 6), lw = w - 4;
    const int b = blockIdx.x;
    const Ids id = ids_of(b);
    const int rt = id.rt, hg = id.hg, brt = id.brt, hq = id.hq;
    const unsigned L0 = lds_addr(lds);
    const int rotq = (b >> 4) & 3;
    const int rotb = ((b >> 5) + 2 * ((b >> 3) & 3)) & 7;
    {
        unsigned xo[4];
#pragma unroll
        for (int i = 0; i < 4; ++i) { const int row = 4 * i + (l >> 4); xo[i] = (row * DM + (((l & 15) ^ (row & 15)) * 8)) * 2 + 4096 - i * 1024; }
        const char* base = (const char*)(A.xa + (size_t)(32 * rt) * DM + ((lw + rotq) & 3) * 128) - 4096;
        glds16x4(base, xo[0], xo[1], xo[2], xo[3], L0 + 8 * lw * 1024);
        glds16x4(base + 16 * DM * 2, xo[0], xo[1], xo[2], xo[3], L0 + (8 * lw + 4) * 1024);
    }
    unsigned wqo[4];
#pragma unroll
    for (int i = 0; i < 4; ++i) { const int p = 8 * lw + i, kr = 64 * (p >> 4) + 4 * (p & 15) + (l >> 4), kk = kr & 63; const int ch = (l & 15) ^ (((kk & 3) << 2) | ((kk >> 2) & 3)); wqo[i] = (kr * 512 + hg * 128 + ch * 8) * 2 + 4096 - i * 1024; }
    auto issue_wq = [&](int it) {
        const char* base = (const char*)A.wq + (size_t)((it - 1 + rotq) & 3) * 128 * 512 * 2 - 4096;
        glds16x4(base, wqo[0], wqo[1], wqo[2], wqo[3], L0 + it * SLOT + 8 * lw * 1024);
        glds16x4(base + 16 * 512 * 2, wqo[0], wqo[1], wqo[2], wqo[3], L0 + it * SLOT + (8 * lw + 4) * 1024);
    };
    issue_wq(1);
    unsigned bo[8];
#pragma unroll
    for (int i = 0; i < 8; ++i) {
        const int p = 8 * lw + i;
        if (lw == 0) { const int row = 8 * p + (l >> 3); bo[i] = ((64 * brt + row) * DM + (((l & 7) ^ ((row >> 1) & 7)) * 8)) * 2 + 4096 - (i & 3) * 1024; }
        else { const int o = (p - 8) * 1024 + l * 16, kr = o / 384, cb = (o % 384) >> 4, g = (cb >> 2) ^ ((kr >> 1) & 1); bo[i] = (kr * 1536 + (g >> 1) * 512 + hq * 64 + (g & 1) * 32 + (cb & 3) * 8) * 2 + 4096 - (i & 3) * 1024; }
    }
    const char* bbase = ((lw == 0) ? (const char*)A.xb : (const char*)A.wqkv) - 4096;
    const int bstride = (lw == 0) ? 64 * 2 : 64 * 1536 * 2;
    auto issue_b = [&](int s) {
        const char* base = bbase + ((s + rotb) & 7) * bstride;
        glds16x4(base, bo[0], bo[1], bo[2], bo[3], L0 + ((s & 3) + 1) * SLOT + 8 * lw * 1024);
        glds16x4(base, bo[4], bo[5], bo[6], bo[7], L0 + ((s & 3) + 1) * SLOT + (8 * lw + 4) * 1024);
    };
    issue_wq(2);
    WAIT_VM(8);
    BAR();
    issue_wq(3);
    WAIT_VM(8);
    BAR();
    issue_b(0);
    WAIT_VM(8);
    BAR();
    issue_wq(4); issue_b(1);
    {
        unsigned ho[4];
#pragma unroll
        for (int i = 0; i < 4; ++i) ho[i] = ((l ^ (4 * lw + i)) * 16) + 4096;
        glds16x4((const char*)(A.xb + (size_t)((brt ? 64 * brt - 16 : 0) + 4 * lw) * DM) - 4096, ho[0], ho[1], ho[2], ho[3], L0 + 4 * lw * 1024);
    }
    WAIT_VM(12);
    BAR();
    issue_b(2);
    WAIT_VM(8);
    BAR();
    issue_b(3);
    const char* kvg = ws + WS_KV;
    const int rot8 = (b >> 3) & 7;
#pragma unroll 1
    for (int s = 0; s < 8; ++s) {
        if (s >= 1) { WAIT_VM(16); }
        BAR();
        if (s <= 3) issue_b(s + 4);
        if (s >= 4 && s <= 6) {
#pragma unroll
            for (int i = 0; i < 2; ++i) { const int p = 32 * (s - 3) + ((8 * lw + 4 * i + 4 * rot8) & 31); glds16x4_sc1(kvg + p * 1024, l * 16, L0 + p * 1024); }
        }
    }
    BAR();
#pragma unroll
    for (int i = 0; i < 2; ++i) { const int p = (8 * lw + 4 * i + 4 * rot8) & 31; glds16x4_sc1(kvg + p * 1024, l * 16, L0 + p * 1024); }
    if (lw < 2) glds16_sc1(kvg + (128 + lw) * 1024 + l * 16, L0 + (128 + lw) * 1024);
    WAIT_VM(0);
    BAR();
}
DI void att_pass(const bf16x8 qa, const bf16x8 qb, float ma, float mb, unsigned ka, unsigned va, f32x16& oa, f32x16& ob) {
    f32x16 na, nb; f32x32 sc; u32x8 pp; u32x4 kf, v0 = {0, 0, 0, 0}, v1 = {0, 0, 0, 0};
    unsigned ns = 8;
#pragma unroll
    for (int i = 0; i < 16; ++i) { na[i] = -ma; nb[i] = -mb; }
    asm volatile(
    "s_mov_b32 s91, 0\n\t"
    "ds_read_b128 %[k], %[ka]\n\t"
    "ds_read_b128 %[v0], %[va]\n\t"
    "v_mov_b32_e32 v252, 0\n\t"
    "v_mov_b32_e32 v253, 0\n\t"
    "v_mov_b32_e32 v254, 0\n\t"
    "v_mov_b32_e32 v255, 0\n\t"
    "s_nop 1\n\t"
    "v_mfma_f32_32x32x16_bf16 %[oa], v[252:255], v[252:255], 0\n\t"
    "v_mfma_f32_32x32x16_bf16 %[ob], v[252:255], v[252:255], 0\n\t"
    "s_waitcnt lgkmcnt(0)\n\t"
    "v_mfma_f32_32x32x16_bf16 v[216:231], %[k], %[qa], %[na]\n\t"
    "s_nop 11\n\t"
    "1:\n\t"
    "v_mfma_f32_32x32x16_bf16 v[232:247], %[k], %[qb], %[nb]\n\t"
    "ds_read_b128 %[k], %[ka] offset:1024\n\t"
    "v_exp_f32_e32 v248, v217\n\t"
    "v_exp_f32_e32 v216, v216\n\t"
    "v_exp_f32_e32 v249, v219\n\t"
    "v_exp_f32_e32 v218, v218\n\t"
    "v_mov_b32_sdwa v248, v216 dst_sel:WORD_0 dst_unused:UNUSED_PRESERVE src0_sel:WORD_1\n\t"
    "v_exp_f32_e32 v250, v221\n\t"
    "v_mfma_f32_32x32x16_bf16 %[ob], %[v1], v[252:255], %[ob]\n\t"
    "ds_read_b128 %[v1], %[va] offset:528\n\t"
    "v_exp_f32_e32 v220, v220\n\t"
    "v_mov_b32_sdwa v249, v218 dst_sel:WORD_0 dst_unused:UNUSED_PRESERVE src0_sel:WORD_1\n\t"
    "v_exp_f32_e32 v251, v223\n\t"
    "v_exp_f32_e32 v222, v222\n\t"
    "v_mov_b32_sdwa v250, v220 dst_sel:WORD_0 dst_unused:UNUSED_PRESERVE src0_sel:WORD_1\n\t"
    "v_exp_f32_e32 v252, v225\n\t"
    "v_mov_b32_sdwa v251, v222 dst_sel:WORD_0 dst_unused:UNUSED_PRESERVE src0_sel:WORD_1\n\t"
    "v_exp_f32_e32 v224, v224\n\t"
    "v_exp_f32_e32 v253, v227\n\t"
    "s_waitcnt lgkmcnt(2)\n\t"
    "v_mfma_f32_32x32x16_bf16 %[oa], %[v0], v[248:251], %[oa]\n\t"
    "v_exp_f32_e32 v226, v226\n\t"
    "v_mov_b32_sdwa v252, v224 dst_sel:WORD_0 dst_unused:UNUSED_PRESERVE src0_sel:WORD_1\n\t"
    "v_exp_f32_e32 v254, v229\n\t"
    "v_exp_f32_e32 v228, v228\n\t"
    "v_mov_b32_sdwa v253, v226 dst_sel:WORD_0 dst_unused:UNUSED_PRESERVE src0_sel:WORD_1\n\t"
    "v_exp_f32_e32 v255, v231\n\t"
    "v_exp_f32_e32 v230, v230\n\t"
    "v_mov_b32_sdwa v254, v228 dst_sel:WORD_0 dst_unused:UNUSED_PRESERVE src0_sel:WORD_1\n\t"
    "v_mov_b32_sdwa v255, v230 dst_sel:WORD_0 dst_unused:UNUSED_PRESERVE src0_sel:WORD_1\n\t"
    "s_waitcnt lgkmcnt(1)\n\t"
    "v_mfma_f32_32x32x16_bf16 v[216:231], %[k], %[qa], %[na]\n\t"
    "v_exp_f32_e32 v248, v233\n\t"
    "v_exp_f32_e32 v232, v232\n\t"
    "v_exp_f32_e32 v249, v235\n\t"
    "v_exp_f32_e32 v234, v234\n\t"
    "v_mov_b32_sdwa v248, v232 dst_sel:WORD_0 dst_unused:UNUSED_PRESERVE src0_sel:WORD_1\n\t"
    "v_exp_f32_e32 v250, v237\n\t"
    "s_waitcnt lgkmcnt(0)\n\t"
    "v_mfma_f32_32x32x16_bf16 %[oa], %[v1], v[252:255], %[oa]\n\t"
    "v_exp_f32_e32 v236, v236\n\t"
    "v_mov_b32_sdwa v249, v234 dst_sel:WORD_0 dst_unused:UNUSED_PRESERVE src0_sel:WORD_1\n\t"
    "v_exp_f32_e32 v251, v239\n\t"
    "v_exp_f32_e32 v238, v238\n\t"
    "v_mov_b32_sdwa v250, v236 dst_sel:WORD_0 dst_unused:UNUSED_PRESERVE src0_sel:WORD_1\n\t"
    "v_exp_f32_e32 v252, v241\n\t"
    "v_mov_b32_sdwa v251, v238 dst_sel:WORD_0 dst_unused:UNUSED_PRESERVE src0_sel:WORD_1\n\t"
    "v_exp_f32_e32 v240, v240\n\t"
    "v_exp_f32_e32 v253, v243\n\t"
    "v_mfma_f32_32x32x16_bf16 %[ob], %[v0], v[248:251], %[ob]\n\t"
    "ds_read_b128 %[v0], %[va] offset:1056\n\t"
    "v_exp_f32_e32 v242, v242\n\t"
    "v_mov_b32_sdwa v252, v240 dst_sel:WORD_0 dst_unused:UNUSED_PRESERVE src0_sel:WORD_1\n\t"
    "v_exp_f32_e32 v254, v245\n\t"
    "v_exp_f32_e32 v244, v244\n\t"
    "v_mov_b32_sdwa v253, v242 dst_sel:WORD_0 dst_unused:UNUSED_PRESERVE src0_sel:WORD_1\n\t"
    "v_exp_f32_e32 v255, v247\n\t"
    "v_exp_f32_e32 v246, v246\n\t"
    "v_mov_b32_sdwa v254, v244 dst_sel:WORD_0 dst_unused:UNUSED_PRESERVE src0_sel:WORD_1\n\t"
    "v_mov_b32_sdwa v255, v246 dst_sel:WORD_0 dst_unused:UNUSED_PRESERVE src0_sel:WORD_1\n\t"
    "v_mfma_f32_32x32x16_bf16 v[232:247], %[k], %[qb], %[nb]\n\t"
    "ds_read_b128 %[k], %[ka] offset:2048\n\t"
    "v_exp_f32_e32 v248, v217\n\t"
    "v_exp_f32_e32 v216, v216\n\t"
    "v_exp_f32_e32 v249, v219\n\t"
    "v_exp_f32_e32 v218, v218\n\t"
    "v_mov_b32_sdwa v248, v216 dst_sel:WORD_0 dst_unused:UNUSED_PRESERVE src0_sel:WORD_1\n\t"
    "v_exp_f32_e32 v250, v221\n\t"
    "v_mfma_f32_32x32x16_bf16 %[ob], %[v1], v[252:255], %[ob]\n\t"
    "ds_read_b128 %[v1], %[va] offset:1584\n\t"
    "v_exp_f32_e32 v220, v220\n\t"
    "v_mov_b32_sdwa v249, v218 dst_sel:WORD_0 dst_unused:UNUSED_PRESERVE src0_sel:WORD_1\n\t"
    "v_exp_f32_e32 v251, v223\n\t"
    "v_exp_f32_e32 v222, v222\n\t"
    "v_mov_b32_sdwa v250, v220 dst_sel:WORD_0 dst_unused:UNUSED_PRESERVE src0_sel:WORD_1\n\t"
    "v_exp_f32_e32 v252, v225\n\t"
    "v_mov_b32_sdwa v251, v222 dst_sel:WORD_0 dst_unused:UNUSED_PRESERVE src0_sel:WORD_1\n\t"
    "v_exp_f32_e32 v224, v224\n\t"
    "v_exp_f32_e32 v253, v227\n\t"
    "s_waitcnt lgkmcnt(2)\n\t"
    "v_mfma_f32_32x32x16_bf16 %[oa], %[v0], v[248:251], %[oa]\n\t"
    "v_exp_f32_e32 v226, v226\n\t"
    "v_mov_b32_sdwa v252, v224 dst_sel:WORD_0 dst_unused:UNUSED_PRESERVE src0_sel:WORD_1\n\t"
    "v_exp_f32_e32 v254, v229\n\t"
    "v_exp_f32_e32 v228, v228\n\t"
    "v_mov_b32_sdwa v253, v226 dst_sel:WORD_0 dst_unused:UNUSED_PRESERVE src0_sel:WORD_1\n\t"
    "v_exp_f32_e32 v255, v231\n\t"
    "v_exp_f32_e32 v230, v230\n\t"
    "v_mov_b32_sdwa v254, v228 dst_sel:WORD_0 dst_unused:UNUSED_PRESERVE src0_sel:WORD_1\n\t"
    "v_mov_b32_sdwa v255, v230 dst_sel:WORD_0 dst_unused:UNUSED_PRESERVE src0_sel:WORD_1\n\t"
    "s_waitcnt lgkmcnt(1)\n\t"
    "v_mfma_f32_32x32x16_bf16 v[216:231], %[k], %[qa], %[na]\n\t"
    "v_exp_f32_e32 v248, v233\n\t"
    "v_exp_f32_e32 v232, v232\n\t"
    "v_exp_f32_e32 v249, v235\n\t"
    "v_exp_f32_e32 v234, v234\n\t"
    "v_mov_b32_sdwa v248, v232 dst_sel:WORD_0 dst_unused:UNUSED_PRESERVE src0_sel:WORD_1\n\t"
    "v_exp_f32_e32 v250, v237\n\t"
    "s_waitcnt lgkmcnt(0)\n\t"
    "v_mfma_f32_32x32x16_bf16 %[oa], %[v1], v[252:255], %[oa]\n\t"
    "v_exp_f32_e32 v236, v236\n\t"
    "v_mov_b32_sdwa v249, v234 dst_sel:WORD_0 dst_unused:UNUSED_PRESERVE src0_sel:WORD_1\n\t"
    "v_exp_f32_e32 v251, v239\n\t"
    "v_exp_f32_e32 v238, v238\n\t"
    "v_mov_b32_sdwa v250, v236 dst_sel:WORD_0 dst_unused:UNUSED_PRESERVE src0_sel:WORD_1\n\t"
    "v_exp_f32_e32 v252, v241\n\t"
    "v_mov_b32_sdwa v251, v238 dst_sel:WORD_0 dst_unused:UNUSED_PRESERVE src0_sel:WORD_1\n\t"
    "v_exp_f32_e32 v240, v240\n\t"
    "v_exp_f32_e32 v253, v243\n\t"
    "v_mfma_f32_32x32x16_bf16 %[ob], %[v0], v[248:251], %[ob]\n\t"
    "ds_read_b128 %[v0], %[va] offset:2112\n\t"
    "v_exp_f32_e32 v242, v242\n\t"
    "v_mov_b32_sdwa v252, v240 dst_sel:WORD_0 dst_unused:UNUSED_PRESERVE src0_sel:WORD_1\n\t"
    "v_exp_f32_e32 v254, v245\n\t"
    "v_exp_f32_e32 v244, v244\n\t"
    "v_mov_b32_sdwa v253, v242 dst_sel:WORD_0 dst_unused:UNUSED_PRESERVE src0_sel:WORD_1\n\t"
    "v_exp_f32_e32 v255, v247\n\t"
    "v_exp_f32_e32 v246, v246\n\t"
    "v_mov_b32_sdwa v254, v244 dst_sel:WORD_0 dst_unused:UNUSED_PRESERVE src0_sel:WORD_1\n\t"
    "v_mov_b32_sdwa v255, v246 dst_sel:WORD_0 dst_unused:UNUSED_PRESERVE src0_sel:WORD_1\n\t"
    "v_mfma_f32_32x32x16_bf16 v[232:247], %[k], %[qb], %[nb]\n\t"
    "ds_read_b128 %[k], %[ka] offset:3072\n\t"
    "v_exp_f32_e32 v248, v217\n\t"
    "v_exp_f32_e32 v216, v216\n\t"
    "v_exp_f32_e32 v249, v219\n\t"
    "v_exp_f32_e32 v218, v218\n\t"
    "v_mov_b32_sdwa v248, v216 dst_sel:WORD_0 dst_unused:UNUSED_PRESERVE src0_sel:WORD_1\n\t"
    "v_exp_f32_e32 v250, v221\n\t"
    "v_mfma_f32_32x32x16_bf16 %[ob], %[v1], v[252:255], %[ob]\n\t"
    "ds_read_b128 %[v1], %[va] offset:2640\n\t"
    "v_exp_f32_e32 v220, v220\n\t"
    "v_mov_b32_sdwa v249, v218 dst_sel:WORD_0 dst_unused:UNUSED_PRESERVE src0_sel:WORD_1\n\t"
    "v_exp_f32_e32 v251, v223\n\t"
    "v_exp_f32_e32 v222, v222\n\t"
    "v_mov_b32_sdwa v250, v220 dst_sel:WORD_0 dst_unused:UNUSED_PRESERVE src0_sel:WORD_1\n\t"
    "v_exp_f32_e32 v252, v225\n\t"
    "v_mov_b32_sdwa v251, v222 dst_sel:WORD_0 dst_unused:UNUSED_PRESERVE src0_sel:WORD_1\n\t"
    "v_exp_f32_e32 v224, v224\n\t"
    "v_exp_f32_e32 v253, v227\n\t"
    "s_waitcnt lgkmcnt(2)\n\t"
    "v_mfma_f32_32x32x16_bf16 %[oa], %[v0], v[248:251], %[oa]\n\t"
    "v_exp_f32_e32 v226, v226\n\t"
    "v_mov_b32_sdwa v252, v224 dst_sel:WORD_0 dst_unused:UNUSED_PRESERVE src0_sel:WORD_1\n\t"
    "v_exp_f32_e32 v254, v229\n\t"
    "v_exp_f32_e32 v228, v228\n\t"
    "v_mov_b32_sdwa v253, v226 dst_sel:WORD_0 dst_unused:UNUSED_PRESERVE src0_sel:WORD_1\n\t"
    "v_exp_f32_e32 v255, v231\n\t"
    "v_exp_f32_e32 v230, v230\n\t"
    "v_mov_b32_sdwa v254, v228 dst_sel:WORD_0 dst_unused:UNUSED_PRESERVE src0_sel:WORD_1\n\t"
    "v_mov_b32_sdwa v255, v230 dst_sel:WORD_0 dst_unused:UNUSED_PRESERVE src0_sel:WORD_1\n\t"
    "s_waitcnt lgkmcnt(1)\n\t"
    "v_mfma_f32_32x32x16_bf16 v[216:231], %[k], %[qa], %[na]\n\t"
    "v_exp_f32_e32 v248, v233\n\t"
    "v_exp_f32_e32 v232, v232\n\t"
    "v_exp_f32_e32 v249, v235\n\t"
    "v_exp_f32_e32 v234, v234\n\t"
    "v_mov_b32_sdwa v248, v232 dst_sel:WORD_0 dst_unused:UNUSED_PRESERVE src0_sel:WORD_1\n\t"
    "v_exp_f32_e32 v250, v237\n\t"
    "s_waitcnt lgkmcnt(0)\n\t"
    "v_mfma_f32_32x32x16_bf16 %[oa], %[v1], v[252:255], %[oa]\n\t"
    "v_exp_f32_e32 v236, v236\n\t"
    "v_mov_b32_sdwa v249, v234 dst_sel:WORD_0 dst_unused:UNUSED_PRESERVE src0_sel:WORD_1\n\t"
    "v_exp_f32_e32 v251, v239\n\t"
    "v_exp_f32_e32 v238, v238\n\t"
    "v_mov_b32_sdwa v250, v236 dst_sel:WORD_0 dst_unused:UNUSED_PRESERVE src0_sel:WORD_1\n\t"
    "v_exp_f32_e32 v252, v241\n\t"
    "v_mov_b32_sdwa v251, v238 dst_sel:WORD_0 dst_unused:UNUSED_PRESERVE src0_sel:WORD_1\n\t"
    "v_exp_f32_e32 v240, v240\n\t"
    "v_exp_f32_e32 v253, v243\n\t"
    "v_mfma_f32_32x32x16_bf16 %[ob], %[v0], v[248:251], %[ob]\n\t"
    "ds_read_b128 %[v0], %[va] offset:3168\n\t"
    "v_exp_f32_e32 v242, v242\n\t"
    "v_mov_b32_sdwa v252, v240 dst_sel:WORD_0 dst_unused:UNUSED_PRESERVE src0_sel:WORD_1\n\t"
    "v_exp_f32_e32 v254, v245\n\t"
    "v_exp_f32_e32 v244, v244\n\t"
    "v_mov_b32_sdwa v253, v242 dst_sel:WORD_0 dst_unused:UNUSED_PRESERVE src0_sel:WORD_1\n\t"
    "v_exp_f32_e32 v255, v247\n\t"
    "v_exp_f32_e32 v246, v246\n\t"
    "v_mov_b32_sdwa v254, v244 dst_sel:WORD_0 dst_unused:UNUSED_PRESERVE src0_sel:WORD_1\n\t"
    "v_mov_b32_sdwa v255, v246 dst_sel:WORD_0 dst_unused:UNUSED_PRESERVE src0_sel:WORD_1\n\t"
    "v_mfma_f32_32x32x16_bf16 v[232:247], %[k], %[qb], %[nb]\n\t"
    "ds_read_b128 %[k], %[ka] offset:4096\n\t"
    "v_exp_f32_e32 v248, v217\n\t"
    "v_exp_f32_e32 v216, v216\n\t"
    "v_exp_f32_e32 v249, v219\n\t"
    "v_exp_f32_e32 v218, v218\n\t"
    "v_mov_b32_sdwa v248, v216 dst_sel:WORD_0 dst_unused:UNUSED_PRESERVE src0_sel:WORD_1\n\t"
    "v_exp_f32_e32 v250, v221\n\t"
    "v_mfma_f32_32x32x16_bf16 %[ob], %[v1], v[252:255], %[ob]\n\t"
    "ds_read_b128 %[v1], %[va] offset:3696\n\t"
    "v_exp_f32_e32 v220, v220\n\t"
    "v_mov_b32_sdwa v249, v218 dst_sel:WORD_0 dst_unused:UNUSED_PRESERVE src0_sel:WORD_1\n\t"
    "v_exp_f32_e32 v251, v223\n\t"
    "v_exp_f32_e32 v222, v222\n\t"
    "v_mov_b32_sdwa v250, v220 dst_sel:WORD_0 dst_unused:UNUSED_PRESERVE src0_sel:WORD_1\n\t"
    "v_exp_f32_e32 v252, v225\n\t"
    "v_mov_b32_sdwa v251, v222 dst_sel:WORD_0 dst_unused:UNUSED_PRESERVE src0_sel:WORD_1\n\t"
    "v_exp_f32_e32 v224, v224\n\t"
    "v_exp_f32_e32 v253, v227\n\t"
    "s_waitcnt lgkmcnt(2)\n\t"
    "v_mfma_f32_32x32x16_bf16 %[oa], %[v0], v[248:251], %[oa]\n\t"
    "v_exp_f32_e32 v226, v226\n\t"
    "v_mov_b32_sdwa v252, v224 dst_sel:WORD_0 dst_unused:UNUSED_PRESERVE src0_sel:WORD_1\n\t"
    "v_exp_f32_e32 v254, v229\n\t"
    "v_exp_f32_e32 v228, v228\n\t"
    "v_mov_b32_sdwa v253, v226 dst_sel:WORD_0 dst_unused:UNUSED_PRESERVE src0_sel:WORD_1\n\t"
    "v_exp_f32_e32 v255, v231\n\t"
    "v_exp_f32_e32 v230, v230\n\t"
    "v_mov_b32_sdwa v254, v228 dst_sel:WORD_0 dst_unused:UNUSED_PRESERVE src0_sel:WORD_1\n\t"
    "v_mov_b32_sdwa v255, v230 dst_sel:WORD_0 dst_unused:UNUSED_PRESERVE src0_sel:WORD_1\n\t"
    "s_waitcnt lgkmcnt(1)\n\t"
    "v_mfma_f32_32x32x16_bf16 v[216:231], %[k], %[qa], %[na]\n\t"
    "v_exp_f32_e32 v248, v233\n\t"
    "v_exp_f32_e32 v232, v232\n\t"
    "v_exp_f32_e32 v249, v235\n\t"
    "v_exp_f32_e32 v234, v234\n\t"
    "v_mov_b32_sdwa v248, v232 dst_sel:WORD_0 dst_unused:UNUSED_PRESERVE src0_sel:WORD_1\n\t"
    "v_exp_f32_e32 v250, v237\n\t"
    "s_waitcnt lgkmcnt(0)\n\t"
    "v_mfma_f32_32x32x16_bf16 %[oa], %[v1], v[252:255], %[oa]\n\t"
    "v_exp_f32_e32 v236, v236\n\t"
    "v_mov_b32_sdwa v249, v234 dst_sel:WORD_0 dst_unused:UNUSED_PRESERVE src0_sel:WORD_1\n\t"
    "v_exp_f32_e32 v251, v239\n\t"
    "v_exp_f32_e32 v238, v238\n\t"
    "v_mov_b32_sdwa v250, v236 dst_sel:WORD_0 dst_unused:UNUSED_PRESERVE src0_sel:WORD_1\n\t"
    "v_exp_f32_e32 v252, v241\n\t"
    "v_mov_b32_sdwa v251, v238 dst_sel:WORD_0 dst_unused:UNUSED_PRESERVE src0_sel:WORD_1\n\t"
    "v_exp_f32_e32 v240, v240\n\t"
    "v_exp_f32_e32 v253, v243\n\t"
    "v_mfma_f32_32x32x16_bf16 %[ob], %[v0], v[248:251], %[ob]\n\t"
    "ds_read_b128 %[v0], %[va] offset:4224\n\t"
    "v_exp_f32_e32 v242, v242\n\t"
    "v_mov_b32_sdwa v252, v240 dst_sel:WORD_0 dst_unused:UNUSED_PRESERVE src0_sel:WORD_1\n\t"
    "v_exp_f32_e32 v254, v245\n\t"
    "v_exp_f32_e32 v244, v244\n\t"
    "v_mov_b32_sdwa v253, v242 dst_sel:WORD_0 dst_unused:UNUSED_PRESERVE src0_sel:WORD_1\n\t"
    "v_exp_f32_e32 v255, v247\n\t"
    "v_exp_f32_e32 v246, v246\n\t"
    "v_mov_b32_sdwa v254, v244 dst_sel:WORD_0 dst_unused:UNUSED_PRESERVE src0_sel:WORD_1\n\t"
    "v_mov_b32_sdwa v255, v246 dst_sel:WORD_0 dst_unused:UNUSED_PRESERVE src0_sel:WORD_1\n\t"
    "v_mfma_f32_32x32x16_bf16 v[232:247], %[k], %[qb], %[nb]\n\t"
    "ds_read_b128 %[k], %[ka] offset:5120\n\t"
    "v_exp_f32_e32 v248, v217\n\t"
    "v_exp_f32_e32 v216, v216\n\t"
    "v_exp_f32_e32 v249, v219\n\t"
    "v_exp_f32_e32 v218, v218\n\t"
    "v_mov_b32_sdwa v248, v216 dst_sel:WORD_0 dst_unused:UNUSED_PRESERVE src0_sel:WORD_1\n\t"
    "v_exp_f32_e32 v250, v221\n\t"
    "v_mfma_f32_32x32x16_bf16 %[ob], %[v1], v[252:255], %[ob]\n\t"
    "ds_read_b128 %[v1], %[va] offset:4752\n\t"
    "v_exp_f32_e32 v220, v220\n\t"
    "v_mov_b32_sdwa v249, v218 dst_sel:WORD_0 dst_unused:UNUSED_PRESERVE src0_sel:WORD_1\n\t"
    "v_exp_f32_e32 v251, v223\n\t"
    "v_exp_f32_e32 v222, v222\n\t"
    "v_mov_b32_sdwa v250, v220 dst_sel:WORD_0 dst_unused:UNUSED_PRESERVE src0_sel:WORD_1\n\t"
    "v_exp_f32_e32 v252, v225\n\t"
    "v_mov_b32_sdwa v251, v222 dst_sel:WORD_0 dst_unused:UNUSED_PRESERVE src0_sel:WORD_1\n\t"
    "v_exp_f32_e32 v224, v224\n\t"
    "v_exp_f32_e32 v253, v227\n\t"
    "s_waitcnt lgkmcnt(2)\n\t"
    "v_mfma_f32_32x32x16_bf16 %[oa], %[v0], v[248:251], %[oa]\n\t"
    "v_exp_f32_e32 v226, v226\n\t"
    "v_mov_b32_sdwa v252, v224 dst_sel:WORD_0 dst_unused:UNUSED_PRESERVE src0_sel:WORD_1\n\t"
    "v_exp_f32_e32 v254, v229\n\t"
    "v_exp_f32_e32 v228, v228\n\t"
    "v_mov_b32_sdwa v253, v226 dst_sel:WORD_0 dst_unused:UNUSED_PRESERVE src0_sel:WORD_1\n\t"
    "v_exp_f32_e32 v255, v231\n\t"
    "v_exp_f32_e32 v230, v230\n\t"
    "v_mov_b32_sdwa v254, v228 dst_sel:WORD_0 dst_unused:UNUSED_PRESERVE src0_sel:WORD_1\n\t"
    "v_mov_b32_sdwa v255, v230 dst_sel:WORD_0 dst_unused:UNUSED_PRESERVE src0_sel:WORD_1\n\t"
    "s_waitcnt lgkmcnt(1)\n\t"
    "v_mfma_f32_32x32x16_bf16 v[216:231], %[k], %[qa], %[na]\n\t"
    "v_exp_f32_e32 v248, v233\n\t"
    "v_exp_f32_e32 v232, v232\n\t"
    "v_exp_f32_e32 v249, v235\n\t"
    "v_exp_f32_e32 v234, v234\n\t"
    "v_mov_b32_sdwa v248, v232 dst_sel:WORD_0 dst_unused:UNUSED_PRESERVE src0_sel:WORD_1\n\t"
    "v_exp_f32_e32 v250, v237\n\t"
    "s_waitcnt lgkmcnt(0)\n\t"
    "v_mfma_f32_32x32x16_bf16 %[oa], %[v1], v[252:255], %[oa]\n\t"
    "v_exp_f32_e32 v236, v236\n\t"
    "v_mov_b32_sdwa v249, v234 dst_sel:WORD_0 dst_unused:UNUSED_PRESERVE src0_sel:WORD_1\n\t"
    "v_exp_f32_e32 v251, v239\n\t"
    "v_exp_f32_e32 v238, v238\n\t"
    "v_mov_b32_sdwa v250, v236 dst_sel:WORD_0 dst_unused:UNUSED_PRESERVE src0_sel:WORD_1\n\t"
    "v_exp_f32_e32 v252, v241\n\t"
    "v_mov_b32_sdwa v251, v238 dst_sel:WORD_0 dst_unused:UNUSED_PRESERVE src0_sel:WORD_1\n\t"
    "v_exp_f32_e32 v240, v240\n\t"
    "v_exp_f32_e32 v253, v243\n\t"
    "v_mfma_f32_32x32x16_bf16 %[ob], %[v0], v[248:251], %[ob]\n\t"
    "ds_read_b128 %[v0], %[va] offset:5280\n\t"
    "v_exp_f32_e32 v242, v242\n\t"
    "v_mov_b32_sdwa v252, v240 dst_sel:WORD_0 dst_unused:UNUSED_PRESERVE src0_sel:WORD_1\n\t"
    "v_exp_f32_e32 v254, v245\n\t"
    "v_exp_f32_e32 v244, v244\n\t"
    "v_mov_b32_sdwa v253, v242 dst_sel:WORD_0 dst_unused:UNUSED_PRESERVE src0_sel:WORD_1\n\t"
    "v_exp_f32_e32 v255, v247\n\t"
    "v_exp_f32_e32 v246, v246\n\t"
    "v_mov_b32_sdwa v254, v244 dst_sel:WORD_0 dst_unused:UNUSED_PRESERVE src0_sel:WORD_1\n\t"
    "v_mov_b32_sdwa v255, v246 dst_sel:WORD_0 dst_unused:UNUSED_PRESERVE src0_sel:WORD_1\n\t"
    "v_mfma_f32_32x32x16_bf16 v[232:247], %[k], %[qb], %[nb]\n\t"
    "ds_read_b128 %[k], %[ka] offset:6144\n\t"
    "v_exp_f32_e32 v248, v217\n\t"
    "v_exp_f32_e32 v216, v216\n\t"
    "v_exp_f32_e32 v249, v219\n\t"
    "v_exp_f32_e32 v218, v218\n\t"
    "v_mov_b32_sdwa v248, v216 dst_sel:WORD_0 dst_unused:UNUSED_PRESERVE src0_sel:WORD_1\n\t"
    "v_exp_f32_e32 v250, v221\n\t"
    "v_mfma_f32_32x32x16_bf16 %[ob], %[v1], v[252:255], %[ob]\n\t"
    "ds_read_b128 %[v1], %[va] offset:5808\n\t"
    "v_exp_f32_e32 v220, v220\n\t"
    "v_mov_b32_sdwa v249, v218 dst_sel:WORD_0 dst_unused:UNUSED_PRESERVE src0_sel:WORD_1\n\t"
    "v_exp_f32_e32 v251, v223\n\t"
    "v_exp_f32_e32 v222, v222\n\t"
    "v_mov_b32_sdwa v250, v220 dst_sel:WORD_0 dst_unused:UNUSED_PRESERVE src0_sel:WORD_1\n\t"
    "v_exp_f32_e32 v252, v225\n\t"
    "v_mov_b32_sdwa v251, v222 dst_sel:WORD_0 dst_unused:UNUSED_PRESERVE src0_sel:WORD_1\n\t"
    "v_exp_f32_e32 v224, v224\n\t"
    "v_exp_f32_e32 v253, v227\n\t"
    "s_waitcnt lgkmcnt(2)\n\t"
    "v_mfma_f32_32x32x16_bf16 %[oa], %[v0], v[248:251], %[oa]\n\t"
    "v_exp_f32_e32 v226, v226\n\t"
    "v_mov_b32_sdwa v252, v224 dst_sel:WORD_0 dst_unused:UNUSED_PRESERVE src0_sel:WORD_1\n\t"
    "v_exp_f32_e32 v254, v229\n\t"
    "v_exp_f32_e32 v228, v228\n\t"
    "v_mov_b32_sdwa v253, v226 dst_sel:WORD_0 dst_unused:UNUSED_PRESERVE src0_sel:WORD_1\n\t"
    "v_exp_f32_e32 v255, v231\n\t"
    "v_exp_f32_e32 v230, v230\n\t"
    "v_mov_b32_sdwa v254, v228 dst_sel:WORD_0 dst_unused:UNUSED_PRESERVE src0_sel:WORD_1\n\t"
    "v_mov_b32_sdwa v255, v230 dst_sel:WORD_0 dst_unused:UNUSED_PRESERVE src0_sel:WORD_1\n\t"
    "s_waitcnt lgkmcnt(1)\n\t"
    "v_mfma_f32_32x32x16_bf16 v[216:231], %[k], %[qa], %[na]\n\t"
    "v_exp_f32_e32 v248, v233\n\t"
    "v_exp_f32_e32 v232, v232\n\t"
    "v_exp_f32_e32 v249, v235\n\t"
    "v_exp_f32_e32 v234, v234\n\t"
    "v_mov_b32_sdwa v248, v232 dst_sel:WORD_0 dst_unused:UNUSED_PRESERVE src0_sel:WORD_1\n\t"
    "v_exp_f32_e32 v250, v237\n\t"
    "s_waitcnt lgkmcnt(0)\n\t"
    "v_mfma_f32_32x32x16_bf16 %[oa], %[v1], v[252:255], %[oa]\n\t"
    "v_exp_f32_e32 v236, v236\n\t"
    "v_mov_b32_sdwa v249, v234 dst_sel:WORD_0 dst_unused:UNUSED_PRESERVE src0_sel:WORD_1\n\t"
    "v_exp_f32_e32 v251, v239\n\t"
    "v_exp_f32_e32 v238, v238\n\t"
    "v_mov_b32_sdwa v250, v236 dst_sel:WORD_0 dst_unused:UNUSED_PRESERVE src0_sel:WORD_1\n\t"
    "v_exp_f32_e32 v252, v241\n\t"
    "v_mov_b32_sdwa v251, v238 dst_sel:WORD_0 dst_unused:UNUSED_PRESERVE src0_sel:WORD_1\n\t"
    "v_exp_f32_e32 v240, v240\n\t"
    "v_exp_f32_e32 v253, v243\n\t"
    "v_mfma_f32_32x32x16_bf16 %[ob], %[v0], v[248:251], %[ob]\n\t"
    "ds_read_b128 %[v0], %[va] offset:6336\n\t"
    "v_exp_f32_e32 v242, v242\n\t"
    "v_mov_b32_sdwa v252, v240 dst_sel:WORD_0 dst_unused:UNUSED_PRESERVE src0_sel:WORD_1\n\t"
    "v_exp_f32_e32 v254, v245\n\t"
    "v_exp_f32_e32 v244, v244\n\t"
    "v_mov_b32_sdwa v253, v242 dst_sel:WORD_0 dst_unused:UNUSED_PRESERVE src0_sel:WORD_1\n\t"
    "v_exp_f32_e32 v255, v247\n\t"
    "v_exp_f32_e32 v246, v246\n\t"
    "v_mov_b32_sdwa v254, v244 dst_sel:WORD_0 dst_unused:UNUSED_PRESERVE src0_sel:WORD_1\n\t"
    "v_mov_b32_sdwa v255, v246 dst_sel:WORD_0 dst_unused:UNUSED_PRESERVE src0_sel:WORD_1\n\t"
    "v_mfma_f32_32x32x16_bf16 v[232:247], %[k], %[qb], %[nb]\n\t"
    "ds_read_b128 %[k], %[ka] offset:7168\n\t"
    "v_exp_f32_e32 v248, v217\n\t"
    "v_exp_f32_e32 v216, v216\n\t"
    "v_exp_f32_e32 v249, v219\n\t"
    "v_exp_f32_e32 v218, v218\n\t"
    "v_mov_b32_sdwa v248, v216 dst_sel:WORD_0 dst_unused:UNUSED_PRESERVE src0_sel:WORD_1\n\t"
    "v_exp_f32_e32 v250, v221\n\t"
    "v_mfma_f32_32x32x16_bf16 %[ob], %[v1], v[252:255], %[ob]\n\t"
    "ds_read_b128 %[v1], %[va] offset:6864\n\t"
    "v_exp_f32_e32 v220, v220\n\t"
    "v_mov_b32_sdwa v249, v218 dst_sel:WORD_0 dst_unused:UNUSED_PRESERVE src0_sel:WORD_1\n\t"
    "v_exp_f32_e32 v251, v223\n\t"
    "v_exp_f32_e32 v222, v222\n\t"
    "v_mov_b32_sdwa v250, v220 dst_sel:WORD_0 dst_unused:UNUSED_PRESERVE src0_sel:WORD_1\n\t"
    "v_exp_f32_e32 v252, v225\n\t"
    "v_mov_b32_sdwa v251, v222 dst_sel:WORD_0 dst_unused:UNUSED_PRESERVE src0_sel:WORD_1\n\t"
    "v_exp_f32_e32 v224, v224\n\t"
    "v_exp_f32_e32 v253, v227\n\t"
    "s_waitcnt lgkmcnt(2)\n\t"
    "v_mfma_f32_32x32x16_bf16 %[oa], %[v0], v[248:251], %[oa]\n\t"
    "v_exp_f32_e32 v226, v226\n\t"
    "v_mov_b32_sdwa v252, v224 dst_sel:WORD_0 dst_unused:UNUSED_PRESERVE src0_sel:WORD_1\n\t"
    "v_exp_f32_e32 v254, v229\n\t"
    "v_exp_f32_e32 v228, v228\n\t"
    "v_mov_b32_sdwa v253, v226 dst_sel:WORD_0 dst_unused:UNUSED_PRESERVE src0_sel:WORD_1\n\t"
    "v_exp_f32_e32 v255, v231\n\t"
    "v_exp_f32_e32 v230, v230\n\t"
    "v_mov_b32_sdwa v254, v228 dst_sel:WORD_0 dst_unused:UNUSED_PRESERVE src0_sel:WORD_1\n\t"
    "v_mov_b32_sdwa v255, v230 dst_sel:WORD_0 dst_unused:UNUSED_PRESERVE src0_sel:WORD_1\n\t"
    "s_waitcnt lgkmcnt(1)\n\t"
    "v_mfma_f32_32x32x16_bf16 v[216:231], %[k], %[qa], %[na]\n\t"
    "v_exp_f32_e32 v248, v233\n\t"
    "v_exp_f32_e32 v232, v232\n\t"
    "v_exp_f32_e32 v249, v235\n\t"
    "v_exp_f32_e32 v234, v234\n\t"
    "v_mov_b32_sdwa v248, v232 dst_sel:WORD_0 dst_unused:UNUSED_PRESERVE src0_sel:WORD_1\n\t"
    "v_exp_f32_e32 v250, v237\n\t"
    "s_waitcnt lgkmcnt(0)\n\t"
    "v_mfma_f32_32x32x16_bf16 %[oa], %[v1], v[252:255], %[oa]\n\t"
    "v_exp_f32_e32 v236, v236\n\t"
    "v_mov_b32_sdwa v249, v234 dst_sel:WORD_0 dst_unused:UNUSED_PRESERVE src0_sel:WORD_1\n\t"
    "v_exp_f32_e32 v251, v239\n\t"
    "v_exp_f32_e32 v238, v238\n\t"
    "v_mov_b32_sdwa v250, v236 dst_sel:WORD_0 dst_unused:UNUSED_PRESERVE src0_sel:WORD_1\n\t"
    "v_exp_f32_e32 v252, v241\n\t"
    "v_mov_b32_sdwa v251, v238 dst_sel:WORD_0 dst_unused:UNUSED_PRESERVE src0_sel:WORD_1\n\t"
    "v_exp_f32_e32 v240, v240\n\t"
    "v_exp_f32_e32 v253, v243\n\t"
    "v_mfma_f32_32x32x16_bf16 %[ob], %[v0], v[248:251], %[ob]\n\t"
    "ds_read_b128 %[v0], %[va] offset:7392\n\t"
    "v_exp_f32_e32 v242, v242\n\t"
    "v_mov_b32_sdwa v252, v240 dst_sel:WORD_0 dst_unused:UNUSED_PRESERVE src0_sel:WORD_1\n\t"
    "v_exp_f32_e32 v254, v245\n\t"
    "v_exp_f32_e32 v244, v244\n\t"
    "v_mov_b32_sdwa v253, v242 dst_sel:WORD_0 dst_unused:UNUSED_PRESERVE src0_sel:WORD_1\n\t"
    "v_exp_f32_e32 v255, v247\n\t"
    "v_exp_f32_e32 v246, v246\n\t"
    "v_mov_b32_sdwa v254, v244 dst_sel:WORD_0 dst_unused:UNUSED_PRESERVE src0_sel:WORD_1\n\t"
    "v_mov_b32_sdwa v255, v246 dst_sel:WORD_0 dst_unused:UNUSED_PRESERVE src0_sel:WORD_1\n\t"
    "v_mfma_f32_32x32x16_bf16 v[232:247], %[k], %[qb], %[nb]\n\t"
    "ds_read_b128 %[k], %[ka] offset:8192\n\t"
    "v_exp_f32_e32 v248, v217\n\t"
    "v_exp_f32_e32 v216, v216\n\t"
    "v_exp_f32_e32 v249, v219\n\t"
    "v_exp_f32_e32 v218, v218\n\t"
    "v_mov_b32_sdwa v248, v216 dst_sel:WORD_0 dst_unused:UNUSED_PRESERVE src0_sel:WORD_1\n\t"
    "v_exp_f32_e32 v250, v221\n\t"
    "v_mfma_f32_32x32x16_bf16 %[ob], %[v1], v[252:255], %[ob]\n\t"
    "ds_read_b128 %[v1], %[va] offset:7920\n\t"
    "v_exp_f32_e32 v220, v220\n\t"
    "v_mov_b32_sdwa v249, v218 dst_sel:WORD_0 dst_unused:UNUSED_PRESERVE src0_sel:WORD_1\n\t"
    "v_exp_f32_e32 v251, v223\n\t"
    "v_exp_f32_e32 v222, v222\n\t"
    "v_mov_b32_sdwa v250, v220 dst_sel:WORD_0 dst_unused:UNUSED_PRESERVE src0_sel:WORD_1\n\t"
    "v_exp_f32_e32 v252, v225\n\t"
    "v_mov_b32_sdwa v251, v222 dst_sel:WORD_0 dst_unused:UNUSED_PRESERVE src0_sel:WORD_1\n\t"
    "v_exp_f32_e32 v224, v224\n\t"
    "v_exp_f32_e32 v253, v227\n\t"
    "s_waitcnt lgkmcnt(2)\n\t"
    "v_mfma_f32_32x32x16_bf16 %[oa], %[v0], v[248:251], %[oa]\n\t"
    "v_exp_f32_e32 v226, v226\n\t"
    "v_mov_b32_sdwa v252, v224 dst_sel:WORD_0 dst_unused:UNUSED_PRESERVE src0_sel:WORD_1\n\t"
    "v_exp_f32_e32 v254, v229\n\t"
    "v_exp_f32_e32 v228, v228\n\t"
    "v_mov_b32_sdwa v253, v226 dst_sel:WORD_0 dst_unused:UNUSED_PRESERVE src0_sel:WORD_1\n\t"
    "v_exp_f32_e32 v255, v231\n\t"
    "v_exp_f32_e32 v230, v230\n\t"
    "v_mov_b32_sdwa v254, v228 dst_sel:WORD_0 dst_unused:UNUSED_PRESERVE src0_sel:WORD_1\n\t"
    "v_mov_b32_sdwa v255, v230 dst_sel:WORD_0 dst_unused:UNUSED_PRESERVE src0_sel:WORD_1\n\t"
    "s_waitcnt lgkmcnt(1)\n\t"
    "v_mfma_f32_32x32x16_bf16 v[216:231], %[k], %[qa], %[na]\n\t"
    "v_exp_f32_e32 v248, v233\n\t"
    "v_exp_f32_e32 v232, v232\n\t"
    "v_exp_f32_e32 v249, v235\n\t"
    "v_exp_f32_e32 v234, v234\n\t"
    "v_mov_b32_sdwa v248, v232 dst_sel:WORD_0 dst_unused:UNUSED_PRESERVE src0_sel:WORD_1\n\t"
    "v_exp_f32_e32 v250, v237\n\t"
    "s_waitcnt lgkmcnt(0)\n\t"
    "v_mfma_f32_32x32x16_bf16 %[oa], %[v1], v[252:255], %[oa]\n\t"
    "v_exp_f32_e32 v236, v236\n\t"
    "v_mov_b32_sdwa v249, v234 dst_sel:WORD_0 dst_unused:UNUSED_PRESERVE src0_sel:WORD_1\n\t"
    "v_exp_f32_e32 v251, v239\n\t"
    "v_exp_f32_e32 v238, v238\n\t"
    "v_mov_b32_sdwa v250, v236 dst_sel:WORD_0 dst_unused:UNUSED_PRESERVE src0_sel:WORD_1\n\t"
    "v_exp_f32_e32 v252, v241\n\t"
    "v_mov_b32_sdwa v251, v238 dst_sel:WORD_0 dst_unused:UNUSED_PRESERVE src0_sel:WORD_1\n\t"
    "v_exp_f32_e32 v240, v240\n\t"
    "v_exp_f32_e32 v253, v243\n\t"
    "v_mfma_f32_32x32x16_bf16 %[ob], %[v0], v[248:251], %[ob]\n\t"
    "ds_read_b128 %[v0], %[va] offset:8448\n\t"
    "v_exp_f32_e32 v242, v242\n\t"
    "v_mov_b32_sdwa v252, v240 dst_sel:WORD_0 dst_unused:UNUSED_PRESERVE src0_sel:WORD_1\n\t"
    "v_exp_f32_e32 v254, v245\n\t"
    "v_exp_f32_e32 v244, v244\n\t"
    "v_mov_b32_sdwa v253, v242 dst_sel:WORD_0 dst_unused:UNUSED_PRESERVE src0_sel:WORD_1\n\t"
    "v_exp_f32_e32 v255, v247\n\t"
    "v_exp_f32_e32 v246, v246\n\t"
    "s_add_u32 s91, s91, 1\n\t"
    "v_mov_b32_sdwa v254, v244 dst_sel:WORD_0 dst_unused:UNUSED_PRESERVE src0_sel:WORD_1\n\t"
    "v_add_u32_e32 %[ka], 8192, %[ka]\n\t"
    "v_mov_b32_sdwa v255, v246 dst_sel:WORD_0 dst_unused:UNUSED_PRESERVE src0_sel:WORD_1\n\t"
    "v_add_u32_e32 %[va], 8448, %[va]\n\t"
    "s_cmp_lt_u32 s91, %[ns]\n\t"
    "s_cbranch_scc1 1b\n\t"
    "s_waitcnt lgkmcnt(0)\n\t"
    "v_mfma_f32_32x32x16_bf16 %[ob], %[v1], v[252:255], %[ob]\n\t"
    "s_nop 15\n\t"
        : [sc] "=&{v[216:247]}"(sc), [pp] "=&{v[248:255]}"(pp),
          [oa] "=&v"(oa), [ob] "=&v"(ob), [k] "=&v"(kf), [v0] "+v"(v0), [v1] "+v"(v1), [ka] "+v"(ka), [va] "+v"(va)
        : [qa] "v"(qa), [qb] "v"(qb), [na] "v"(na), [nb] "v"(nb), [ns] "s"(ns)
        : "memory", "scc", "s91");
}
DI float nonfinite_probe(const f32x16& o, float t) {
#pragma unroll
    for (int i = 0; i < 9; ++i) t = __builtin_fmaf(o[i], 0.f, t);
    return t;
}

DI void attention_wave(const Args& A, char* lds, u64 tag, const bf16x8 qa, const bf16x8 qb, float ma, float mb) {
    char* ws = A.ws;
    int tid = threadIdx.x; asm volatile("" : "+v"(tid));
    const int l = tid & 63, c = __builtin_amdgcn_readfirstlane(tid >> 6);
    const int h = l >> 5, r = l & 31;
    const Ids id = ids_of(blockIdx.x);
    const unsigned L0 = lds_addr(lds);
    const unsigned kaddr = L0 + l * 16;
    const unsigned vaddr = r < 16 ? L0 + 65536 + h * 256 + r * 16 : ((r == 16 || r == 20) ? L0 + 65536 + 512 : 0x100000u);
    f32x16 oa, ob;
    const __amdgpu_buffer_rsrc_t rsO = __builtin_amdgcn_make_buffer_rsrc(ws + WS_OA, 0, S * DM * 2, 0x00020000);
    const int fr = 8 * id.hg + 2 * c;
    u32x4 ova, ovb;
#pragma unroll 1
    for (int attempt = 0; attempt < 2; ++attempt) {
        att_pass(qa, qb, ma, mb, kaddr, vaddr, oa, ob);
        ova = pack8(oa, 0, __builtin_amdgcn_rcpf(oa[8])); ovb = pack8(ob, 0, __builtin_amdgcn_rcpf(ob[8]));
        bstore_sc1(ova, rsO, l * 16, (id.rt * 32 + fr) * 1024); bstore_sc1(ovb, rsO, l * 16, (id.rt * 32 + fr + 1) * 1024);
        const float probe = nonfinite_probe(ob, nonfinite_probe(oa, 0.f));
        if (attempt == 1 || __all(probe == 0.f)) break;
        const f32x16 zero = {};
        float xa = -INFINITY, xb = -INFINITY;
#pragma unroll 1
        for (int t = 0; t < 64; ++t) {
            const bf16x8 kt = *(const bf16x8*)(lds + l * 16 + t * 1024);
            xa = fmaxf(xa, max16(MFMA32(kt, qa, zero))); xb = fmaxf(xb, max16(MFMA32(kt, qb, zero)));
        }
        ma = halfmax(xa); mb = halfmax(xb);
    }
    WAIT_VM(0);
    if (l == 0) __hip_atomic_store((GLBP u64*)(ws + WS_FLAG) + 1536 + (id.rt * 4 + id.hg) * 4 + c, tag, RLX_AGENT);
    BAR();
    *(u32x4*)(lds + fr * 1024 + l * 16) = ova; *(u32x4*)(lds + (fr + 1) * 1024 + l * 16) = ovb;
    BAR();
}

template <typename G, typename F>
DI void stream_w(const u16* wo, int col0, char* ring, unsigned ringL, int l, G&& after_first_issue, F&& consume) {
    const int h = l >> 5, blk = (l >> 4) & 1, q4 = (l & 15) >> 2, p4 = l & 3, sg = ((p4 & 1) << 1) | (p4 >> 1);
    const unsigned voff = ((l >> 2) * 512 + col0 + (l & 3) * 8) * 2;
    const char* rd = ring + (8 * h + q4) * 64 + blk * 32 + sg * 8;
    auto issue = [&](int ch) { glds16x2((const char*)wo + (size_t)ch * 32 * 1024, voff, voff + 15 * 1024, ringL + (ch % 3) * 2048); };
    issue(0); issue(1); issue(2);
    after_first_issue();
#pragma unroll
    for (int ch = 0; ch < 16; ++ch) {
        if (ch <= 13) { WAIT_VM(4); } else if (ch == 14) { WAIT_VM(2); } else { WAIT_VM(0); }
        const char* p = rd + (ch % 3) * 2048;
        const bf16x8 f0 = tr_frag(p, p + 256), f1 = tr_frag(p + 1024, p + 1024 + 256);
        asm volatile("s_waitcnt lgkmcnt(0)" : "+v"(const_cast<bf16x8&>(f0)), "+v"(const_cast<bf16x8&>(f1)) :: "memory");
        if (ch + 3 < 16) issue(ch + 3);
        consume(2 * ch, f0); consume(2 * ch + 1, f1);
    }
}
template <bool WT>
DI void store_out(const f32x16& acc, u16* out, int rt, int col, int l) {
    const int off = ((32 * rt + (l & 31)) * 1024 + col + 8 * (l >> 5)) * 2;
    if (WT) {
        const __amdgpu_buffer_rsrc_t rs = __builtin_amdgcn_make_buffer_rsrc(out, 0, S * 1024 * 2, 0x00020000);
        bstore_sc1(pack8(acc, 0, 1.f), rs, off, 0); bstore_sc1(pack8(acc, 8, 1.f), rs, off + 32, 0);
    } else {
        *(u32x4*)((char*)out + off) = pack8(acc, 0, 1.f); *(u32x4*)((char*)out + off + 32) = pack8(acc, 8, 1.f);
    }
}

DI void branch_wave(const Args& A, char* lds, u64 tag) {
    char* ws = A.ws;
    const int tid = threadIdx.x, l = tid & 63, c = __builtin_amdgcn_readfirstlane(tid >> 6) - 4;
    const Ids id = ids_of(blockIdx.x);
    const int rt = id.rt, hg = id.hg, brt = id.brt, hq = id.hq;
    const unsigned L0 = lds_addr(lds);
    GLBP u64* flags = (GLBP u64*)(ws + WS_FLAG);
    const __amdgpu_buffer_rsrc_t rsB = __builtin_amdgcn_make_buffer_rsrc(ws + WS_OB, 0, S * DM * 2, 0x00020000);
    __builtin_amdgcn_s_setprio(1);
    {
        const char* qt = lds + QT;
        const int lr = 16 * c + (l >> 2), hd = l & 3, grow = 64 * brt + lr;
        auto ld = [&](int row, int chunk) { return *(const u32x4*)(qt + row * 384 + ((chunk ^ (row & 7)) * 16)); };
        const u32x4 q0 = ld(16 + lr, 2 * hd), q1 = ld(16 + lr, 2 * hd + 1);
        float lg[8];
#pragma unroll
        for (int wv = 0; wv < 8; ++wv) {
            const u32x4 k0 = ld(16 + lr - 2 * wv, 8 + 2 * hd), k1 = ld(16 + lr - 2 * wv, 8 + 2 * hd + 1);
            float s0 = 0.f, s1 = 0.f;
#pragma unroll
            for (int i = 0; i < 4; ++i) { s0 = dot2(q0[i], k0[i], s0); s1 = dot2(q1[i], k1[i], s1); }
            lg[wv] = (grow - 2 * wv < 0) ? -INFINITY : (s0 + s1) * QSCALE;
        }
        float m = lg[0];
#pragma unroll
        for (int wv = 1; wv < 8; ++wv) m = fmaxf(m, lg[wv]);
        float pw[8], ls = 0.f;
#pragma unroll
        for (int wv = 0; wv < 8; ++wv) { pw[wv] = __builtin_amdgcn_exp2f(lg[wv] - m); ls += pw[wv]; }
        const float inv = __builtin_amdgcn_rcpf(ls);
        float o[16];
#pragma unroll
        for (int i = 0; i < 16; ++i) o[i] = 0.f;
#pragma unroll
        for (int wp = 0; wp < 4; ++wp) {
            const unsigned pp = pk_bf16(pw[2 * wp] * inv, pw[2 * wp + 1] * inv);
            const int ra = 16 + lr - 4 * wp, rb = ra - 2;
            const u32x4 a0 = ld(ra, 16 + 2 * hd), a1 = ld(ra, 16 + 2 * hd + 1), c0 = ld(rb, 16 + 2 * hd), c1 = ld(rb, 16 + 2 * hd + 1);
#pragma unroll
            for (int i = 0; i < 4; ++i) {
                o[2 * i] = dot2(__builtin_amdgcn_perm(c0[i], a0[i], 0x05040100u), pp, o[2 * i]);
                o[2 * i + 1] = dot2(__builtin_amdgcn_perm(c0[i], a0[i], 0x07060302u), pp, o[2 * i + 1]);
                o[8 + 2 * i] = dot2(__builtin_amdgcn_perm(c1[i], a1[i], 0x05040100u), pp, o[8 + 2 * i]);
                o[8 + 2 * i + 1] = dot2(__builtin_amdgcn_perm(c1[i], a1[i], 0x07060302u), pp, o[8 + 2 * i + 1]);
            }
        }
        u32x4 r0, r1;
#pragma unroll
        for (int i = 0; i < 4; ++i) { r0[i] = pk_bf16(o[2 * i], o[2 * i + 1]); r1[i] = pk_bf16(o[8 + 2 * i], o[8 + 2 * i + 1]); }
        const int off = ((grow >> 5) * 32 + 4 * hq + hd) * 1024 + (grow & 31) * 16;
        bstore_sc1(r0, rsB, off, 0); bstore_sc1(r1, rsB, off + 512, 0);
    }
    WAIT_VM(0);
    if (l == 0) __hip_atomic_store(flags + 512 + (8 * brt + hq) * 4 + c, tag, RLX_AGENT);
    poll_flags(l < 32 ? flags + 512 + 32 * (rt >> 1) + l : flags + 512 + (8 * brt + hq) * 4 + (l & 3), l < 36, tag);
    char* ring = lds + QT + c * 7680; const unsigned ringL = L0 + QT + c * 7680;
    const int col0 = 128 * hg + 32 * c;
    {
        u32x4 xb[32];
        f32x16 acc = {};
        stream_w(A.wob, col0, ring, ringL, l, [&]() {
#pragma unroll
            for (int s = 0; s < 32; ++s) xb[s] = bload_sc1(rsB, l * 16, (rt * 32 + s) * 1024);
#pragma unroll
            for (int s = 0; s < 32; ++s) asm volatile("" : "+v"(xb[s]));
        }, [&](int ks, const bf16x8& f) { acc = MFMA32(f, __builtin_bit_cast(bf16x8, xb[ks]), acc); });
        store_out<true>(acc, A.out, rt, 512 + col0, l);
    }
    bf16x8 wa[32];
    stream_w(A.woa, col0, ring, ringL, l, [&]() {}, [&](int ks, const bf16x8& f) { wa[ks] = f; });
    __builtin_amdgcn_s_setprio(0);
    BAR();
    u32x4 t[6];
    {
        const int f = (8 * (hg + 1) + 6 * c + (l < 6 ? l : 0)) & 31;
        poll_flags(flags + 1536 + (rt * 4 + (f >> 3)) * 4 + ((f & 7) >> 1), l < 6, tag);
        const __amdgpu_buffer_rsrc_t rsO = __builtin_amdgcn_make_buffer_rsrc(ws + WS_OA, 0, S * DM * 2, 0x00020000);
#pragma unroll
        for (int i = 0; i < 6; ++i) t[i] = bload_sc1(rsO, l * 16, (rt * 32 + ((8 * (hg + 1) + 6 * c + i) & 31)) * 1024);
    }
#pragma unroll
    for (int i = 0; i < 6; ++i) *(u32x4*)(lds + ((8 * (hg + 1) + 6 * c + i) & 31) * 1024 + l * 16) = t[i];
    BAR();
    f32x16 acc = {};
    {
        bf16x8 xf[32];
#pragma unroll
        for (int s = 0; s < 32; ++s) xf[s] = *(const bf16x8*)(lds + s * 1024 + l * 16);
#pragma unroll
        for (int s = 0; s < 32; ++s) acc = MFMA32(wa[s], xf[s], acc);
        __builtin_amdgcn_sched_group_barrier(0x100, 4, 0);
#pragma unroll
        for (int s = 0; s < 28; ++s) { __builtin_amdgcn_sched_group_barrier(0x8, 1, 0); __builtin_amdgcn_sched_group_barrier(0x100, 1, 0); }
        __builtin_amdgcn_sched_group_barrier(0x8, 4, 0);
    }
    store_out<false>(acc, A.out, rt, col0, l);
}

__global__ __launch_bounds__(512, 2) void fused_kernel(Args A) {
    extern __shared__ __attribute__((aligned(16))) char lds[];
    const u64 tag = *(const u64*)(A.ws + WS_EPOCH) + 1;
    if (__builtin_amdgcn_readfirstlane(threadIdx.x >> 6) < 4) {
        bf16x8 qa, qb; float ma, mb;
        front_compute(A, lds, tag, qa, qb, ma, mb);
        attention_wave(A, lds, tag, qa, qb, ma, mb);
    } else {
        front_stage(A, lds, tag);
        branch_wave(A, lds, tag);
    }
}
extern "C" void kernel_launch(void* const* d_in, const int* in_sizes, int n_in, void* d_out, int out_size, void* d_ws, size_t ws_size, hipStream_t stream) {
    Args a;
    a.xa = (const u16*)d_in[0]; a.wq = (const u16*)d_in[1]; a.wk = (const u16*)d_in[2]; a.wv = (const u16*)d_in[3]; a.woa = (const u16*)d_in[4];
    a.xb = (const u16*)d_in[5]; a.wqkv = (const u16*)d_in[6]; a.wob = (const u16*)d_in[7]; a.ws = (char*)d_ws; a.out = (u16*)d_out;
    constexpr int LDSB = 163840;
    (void)hipFuncSetAttribute((const void*)fused_kernel, hipFuncAttributeMaxDynamicSharedMemorySize, LDSB);
    fused_kernel<<<256, 512, LDSB, stream>>>(a);
}
```
